```python
import torch

def _softmax(x, axis=-1):
    return torch.softmax(x, dim=axis)
def _gelu(x):
    return torch.nn.functional.gelu(x, approximate="tanh")
def _silu(x):
    return torch.nn.functional.silu(x)
def _sigmoid(x):
    return torch.sigmoid(x)
def _layernorm(x, axis=-1, eps=1e-5):
    m = x.mean(dim=axis, keepdim=True)
    v = x.var(dim=axis, keepdim=True, unbiased=False)
    return (x - m) / torch.sqrt(v + eps)
def _scatter_add(tgt, idx, src, axis=0):
    return tgt.clone().index_add_(axis, idx, src)
def _segment_sum(src, seg, n):
    out = torch.zeros((n,) + src.shape[1:], dtype=src.dtype)
    return out.index_add_(0, seg, src)
def _pad(x, pw):
    flat = tuple(v for pair in reversed(pw) for v in pair)
    return torch.nn.functional.pad(x, flat)

kernel_name = 'concat_heads__gqa_block_cached__gqa_block_cached__near_ridge'

def setup_inputs():

    _w = (lambda a: (lambda t: t.to(torch.bfloat16) if t.is_floating_point() else t)(torch.as_tensor(a)))
    def _A_setup():
        B, Sq, D = 16, 144, 512
        Hq, Hkv, Sk, Dh = 32, 32, 512, 128
        assert Hq % Hkv == 0, (Hq, Hkv)
        return {
            "x": _w((lambda *s: torch.randn(*s, dtype=torch.bfloat16) * 0.8)(B, Sq, D)),
            "k_cache": _w((lambda *s: torch.randn(*s, dtype=torch.bfloat16) * 0.8)(B, Hkv, Sk, Dh)),
            "v_cache": _w((lambda *s: torch.randn(*s, dtype=torch.bfloat16) * 0.05)(B, Hkv, Sk, Dh)),
            "wq": _w((lambda *s: torch.randn(*s, dtype=torch.bfloat16) * 0.8)(D, Hq * Dh) * (D ** -0.5)),
            "wo": _w((lambda *s: torch.randn(*s, dtype=torch.bfloat16) * 0.05)(Hq * Dh, D) * ((Hq * Dh) ** -0.5)),
        }
    _A_in = _A_setup()

    def _B_setup():
        B, Sq, D = 16, 144, 512
        Hq, Hkv, Sk, Dh = 32, 32, 512, 128
        assert Hq % Hkv == 0, (Hq, Hkv)
        return {
            "x": _w((lambda *s: torch.randn(*s, dtype=torch.bfloat16) * 0.8)(B, Sq, D)),
            "k_cache": _w((lambda *s: torch.randn(*s, dtype=torch.bfloat16) * 0.8)(B, Hkv, Sk, Dh)),
            "v_cache": _w((lambda *s: torch.randn(*s, dtype=torch.bfloat16) * 0.05)(B, Hkv, Sk, Dh)),
            "wq": _w((lambda *s: torch.randn(*s, dtype=torch.bfloat16) * 0.8)(D, Hq * Dh) * (D ** -0.5)),
            "wo": _w((lambda *s: torch.randn(*s, dtype=torch.bfloat16) * 0.05)(Hq * Dh, D) * ((Hq * Dh) ** -0.5)),
        }
    _B_in = _B_setup()

    return {
        "A_x": _A_in["x"],
        "A_k_cache": _A_in["k_cache"],
        "A_v_cache": _A_in["v_cache"],
        "A_wq": _A_in["wq"],
        "A_wo": _A_in["wo"],
        "B_x": _B_in["x"],
        "B_k_cache": _B_in["k_cache"],
        "B_v_cache": _B_in["v_cache"],
        "B_wq": _B_in["wq"],
        "B_wo": _B_in["wo"],
    }

def reference(A_x, A_k_cache, A_v_cache, A_wq, A_wo, B_x, B_k_cache, B_v_cache, B_wq, B_wo):
    def _A_ref(x, k_cache, v_cache, wq, wo):
        B, Sq, D = x.shape
        Hq, Dh = 32, 128
        Hkv = k_cache.shape[1]
        rep = Hq // Hkv
        q = (lambda x, *d: x.permute(*d))((x @ wq).reshape(B, Sq, Hq, Dh), 0, 2, 1, 3)
        k = (lambda x, n, axis: torch.repeat_interleave(x, n, dim=axis))(k_cache, rep, axis=1)
        v = (lambda x, n, axis: torch.repeat_interleave(x, n, dim=axis))(v_cache, rep, axis=1)
        scores = torch.einsum("bhqd,bhkd->bhqk", q, k) * (Dh ** -0.5)
        p = _softmax(scores, axis=-1)
        o = torch.einsum("bhqk,bhkd->bhqd", p, v)
        return (lambda x, *d: x.permute(*d))(o, 0, 2, 1, 3).reshape(B, Sq, Hq * Dh) @ wo

    def _B_ref(x, k_cache, v_cache, wq, wo):
        B, Sq, D = x.shape
        Hq, Dh = 32, 128
        Hkv = k_cache.shape[1]
        rep = Hq // Hkv
        q = (lambda x, *d: x.permute(*d))((x @ wq).reshape(B, Sq, Hq, Dh), 0, 2, 1, 3)
        k = (lambda x, n, axis: torch.repeat_interleave(x, n, dim=axis))(k_cache, rep, axis=1)
        v = (lambda x, n, axis: torch.repeat_interleave(x, n, dim=axis))(v_cache, rep, axis=1)
        scores = torch.einsum("bhqd,bhkd->bhqk", q, k) * (Dh ** -0.5)
        p = _softmax(scores, axis=-1)
        o = torch.einsum("bhqk,bhkd->bhqd", p, v)
        return (lambda x, *d: x.permute(*d))(o, 0, 2, 1, 3).reshape(B, Sq, Hq * Dh) @ wo

    a = _A_ref(A_x, A_k_cache, A_v_cache, A_wq, A_wo)
    b = _B_ref(B_x, B_k_cache, B_v_cache, B_wq, B_wo)
    return torch.concatenate([a, b], axis=-1)
```

```cpp
#include <hip/hip_runtime.h>
#include <hip/hip_bf16.h>
#include <cstdint>

typedef __bf16   bf16x8 __attribute__((ext_vector_type(8)));
typedef __bf16   bf16x4 __attribute__((ext_vector_type(4)));
typedef __bf16   bf16x2 __attribute__((ext_vector_type(2)));
typedef short    s16x4  __attribute__((ext_vector_type(4)));
typedef float    f32x16 __attribute__((ext_vector_type(16)));
typedef float    f32x4  __attribute__((ext_vector_type(4)));
typedef float    f32x2  __attribute__((ext_vector_type(2)));
typedef unsigned u32x4  __attribute__((ext_vector_type(4)));
typedef unsigned u32x2  __attribute__((ext_vector_type(2)));
typedef __attribute__((address_space(3))) char* lds_ptr;
typedef __attribute__((address_space(3))) const char* lds_cptr;
typedef __amdgpu_buffer_rsrc_t rsrc_t;

#define DI __device__ __forceinline__
#define MFMA32(a, b, c) __builtin_amdgcn_mfma_f32_32x32x16_bf16((a), (b), (c), 0, 0, 0)
#define MFMA16(a, b, c) __builtin_amdgcn_mfma_f32_16x16x32_bf16((a), (b), (c), 0, 0, 0)
#define WAIT_BAR(N) asm volatile("s_waitcnt vmcnt(" #N ") lgkmcnt(0)\n\ts_barrier" ::: "memory")
#define SBAR() __builtin_amdgcn_sched_barrier(0)

constexpr int NB = 16, SQ = 144, DM = 512, NH = 32, SK = 512, DH = 128;
constexpr float C2 = 0.08838834764831845f * 1.4426950408889634f;
constexpr float THR = 8.0f;
constexpr int SLOT = 16384, MISC = 4 * SLOT, LDS1 = 5 * SLOT;
constexpr int OITEM = 36 * 1024;

DI unsigned rfl(unsigned x) { return __builtin_amdgcn_readfirstlane(x); }
DI unsigned cvtpk(float lo, float hi) { f32x2 v = {lo, hi}; bf16x2 b = __builtin_convertvector(v, bf16x2); return __builtin_bit_cast(unsigned, b); }
DI rsrc_t mkrsrc(const void* p, unsigned bytes) { return __builtin_amdgcn_make_buffer_rsrc((void*)p, (short)0, (int)bytes, 0x00020000); }
template <bool NT> DI void glds(unsigned m0v, unsigned voff, rsrc_t srd, unsigned soff) {
  if constexpr (NT) asm volatile("s_mov_b32 m0, %0\n\ts_nop 0\n\tbuffer_load_dwordx4 %1, %2, %3 offen nt lds" :: "s"(m0v), "v"(voff), "s"(srd), "s"(soff) : "memory");
  else              asm volatile("s_mov_b32 m0, %0\n\ts_nop 0\n\tbuffer_load_dwordx4 %1, %2, %3 offen lds" :: "s"(m0v), "v"(voff), "s"(srd), "s"(soff) : "memory");
}
DI bf16x8 ldsb128(lds_cptr p) { return *(const __attribute__((address_space(3))) bf16x8*)p; }
DI bf16x4 ldstr(lds_cptr p) { return __builtin_bit_cast(bf16x4, __builtin_amdgcn_ds_read_tr16_b64_v4i16((__attribute__((address_space(3))) s16x4*)p)); }
DI bf16x8 ldstr2(lds_cptr p, int off2) { bf16x4 a = ldstr(p), b = ldstr(p + off2); return __builtin_shufflevector(a, b, 0, 1, 2, 3, 4, 5, 6, 7); }
DI bf16x8 pack8(const f32x16& x, int s) {
  u32x4 w = {cvtpk(x[8 * s + 0], x[8 * s + 1]), cvtpk(x[8 * s + 2], x[8 * s + 3]), cvtpk(x[8 * s + 4], x[8 * s + 5]), cvtpk(x[8 * s + 6], x[8 * s + 7])};
  return __builtin_bit_cast(bf16x8, w);
}
DI float swapmax32(float m) { auto r = __builtin_amdgcn_permlane32_swap(__float_as_uint(m), __float_as_uint(m), false, false); return fmaxf(__uint_as_float(r[0]), __uint_as_float(r[1])); }
DI float swapadd32(float m) { auto r = __builtin_amdgcn_permlane32_swap(__float_as_uint(m), __float_as_uint(m), false, false); return __uint_as_float(r[0]) + __uint_as_float(r[1]); }
DI float swapmax16(float m) { auto r = __builtin_amdgcn_permlane16_swap(__float_as_uint(m), __float_as_uint(m), false, false); return fmaxf(__uint_as_float(r[0]), __uint_as_float(r[1])); }
DI float swapadd16(float m) { auto r = __builtin_amdgcn_permlane16_swap(__float_as_uint(m), __float_as_uint(m), false, false); return __uint_as_float(r[0]) + __uint_as_float(r[1]); }

struct Ptrs { const void* x[2]; const void* k[2]; const void* v[2]; const void* wq[2]; const void* wo[2]; void* out; void* ws; };

__global__ __launch_bounds__(256, 2) void attn_kernel(Ptrs P) {
  extern __shared__ __attribute__((aligned(16))) char smem[];
  const int tid = threadIdx.x, lane = tid & 63;
  const unsigned w = rfl(tid >> 6);
  const int r32 = lane & 31, hi = lane >> 5, r16 = lane & 15, g4 = lane >> 4, qp = (lane & 15) >> 2, p4 = lane & 3, blk = (lane >> 4) & 1;
  const unsigned bid = blockIdx.x, xcd = bid & 7, j = bid >> 3, round = j >> 6, jj = j & 63;
  const unsigned br = xcd >> 2, b = 8 * ((xcd >> 1) & 1) + (jj & 7), h = 8 * (2 * round + (xcd & 1)) + (jj >> 3);
  const unsigned item = (br * NB + b) * NH + h;
  const rsrc_t srdW = mkrsrc((const char*)P.wq[br] + h * 256, DM * NH * DH * 2 - h * 256);
  const rsrc_t srdX = mkrsrc((const char*)P.x[br] + (size_t)b * SQ * DM * 2, SQ * DM * 2);
  const rsrc_t srdK = mkrsrc((const char*)P.k[br] + (size_t)(b * NH + h) * SK * DH * 2, SK * DH * 2);
  const rsrc_t srdV = mkrsrc((const char*)P.v[br] + (size_t)(b * NH + h) * SK * DH * 2, SK * DH * 2);
  const unsigned lds0 = (unsigned)(uintptr_t)smem;
  const lds_cptr L = (lds_cptr)smem;

  const unsigned lr = (lane >> 2) & 7, hf = w & 1, par = w >> 1;
  const unsigned chP = 4 * (2 * hf + hi) + p4;
  const unsigned chK = 4 * (2 * hf + hi) + (p4 ^ ((2 * par + blk) & 3));
  const unsigned voW = 8192u * (8 * par + lr) + 16 * chP;
  const unsigned voK = 256u * (8 * par + lr) + 16 * chK;
  const unsigned voV = 256u * (8 * par + lr) + 16 * chP;
  const unsigned xrX = (2 * hf + blk) & 3;
  const unsigned voX = 1024u * (64 * par + 8 * hf + lr) + 16 * (4 * hi + (p4 ^ xrX));
  const unsigned voE = 1024u * (128 + 8 * hf + lr) + 16 * (4 * hi + (p4 ^ xrX)) + 512 * par;
  const unsigned m0T = lds0 + 2048 * par + 1024 * hf;
  const unsigned m0X = lds0 + 1024 * (8 * par + hf);
  const unsigned m0E = lds0 + MISC + 8192 * hf + 4096 * par;
  auto ldW = [&](int step, int slot) { _Pragma("unroll") for (int i = 0; i < 4; ++i) glds<false>(m0T + slot * SLOT + 4096 * i, voW, srdW, (unsigned)(step * 64 * 8192 + i * 16 * 8192)); };
  auto ldX = [&](int step, int slot) { _Pragma("unroll") for (int i = 0; i < 4; ++i) glds<false>(m0X + slot * SLOT + 2048 * i, voX, srdX, (unsigned)(step * 128 + i * 16 * 1024)); };
  auto ldK = [&](int t, int slot) { _Pragma("unroll") for (int i = 0; i < 4; ++i) glds<true>(m0T + slot * SLOT + 4096 * i, voK, srdK, (unsigned)(t * 16384 + i * 4096)); };
  auto ldV = [&](int t, int slot) { _Pragma("unroll") for (int i = 0; i < 4; ++i) glds<true>(m0T + slot * SLOT + 4096 * i, voV, srdV, (unsigned)(t * 16384 + i * 4096)); };

  asm volatile("s_nop 4" ::: "memory");
  _Pragma("unroll") for (int i = 0; i < 4; ++i) glds<false>(m0E + 1024 * i, voE, srdX, 128u * i);
  ldW(0, 0); ldX(0, 1); ldW(1, 2); ldX(1, 3);

  const lds_cptr bW  = L + 2048 * hi + 64 * qp + 32 * blk + 16 * (lane & 1) + 8 * ((lane >> 1) & 1);
  const lds_cptr bWx = L + 2048 * g4 + 512 * w + 64 * qp + 8 * p4;
  const int xq = (r32 >> 2) & 3;
  const lds_cptr bX0 = L + 1024 * (4 * w + (r32 >> 3)) + 64 * (r32 & 7) + 16 * ((0 + hi) ^ xq);
  const lds_cptr bX1 = L + 1024 * (4 * w + (r32 >> 3)) + 64 * (r32 & 7) + 16 * ((2 + hi) ^ xq);
  const lds_cptr bE  = L + MISC + 8192 * (r16 >> 3) + 64 * (r16 & 7) + 16 * (g4 ^ ((r16 >> 2) & 3));

  f32x16 qacc[4] = {}; f32x4 qx[2] = {};
#pragma unroll 1
  for (int st2 = 0; st2 < 4; ++st2) {
#pragma unroll
    for (int pr = 0; pr < 2; ++pr) {
      const int step = 2 * st2 + pr;
      if (step == 0) { WAIT_BAR(8); }
      else {
        WAIT_BAR(0);
        if (step < 7) { ldW(step + 1, 2 * (pr ^ 1)); ldX(step + 1, 2 * (pr ^ 1) + 1); }
        else { ldK(0, 0); ldV(0, 1); }
      }
      const int ws = 2 * pr * SLOT, xs = ws + SLOT;
#pragma unroll
      for (int kk = 0; kk < 4; ++kk) {
        const bf16x8 xb = ldsb128(((kk & 1) ? bX1 : bX0) + xs + 512 * (kk >> 1));
#pragma unroll
        for (int dt = 0; dt < 4; ++dt) qacc[dt] = MFMA32(ldstr2(bW + ws + 4096 * kk + 512 * dt, 256), xb, qacc[dt]);
      }
#pragma unroll
      for (int s = 0; s < 2; ++s) {
        const bf16x8 xe = ldsb128(bE + 512 * (2 * step + s));
#pragma unroll
        for (int u = 0; u < 2; ++u) qx[u] = MFMA16(ldstr2(bWx + ws + 8192 * s + 32 * u, 256), xe, qx[u]);
      }
    }
  }
  bf16x8 qf[8];
#pragma unroll
  for (int dt = 0; dt < 4; ++dt) { qf[2 * dt] = pack8(qacc[dt], 0); qf[2 * dt + 1] = pack8(qacc[dt], 1); }
#pragma unroll
  for (int u = 0; u < 2; ++u) {
    u32x2 v = {cvtpk(qx[u][0], qx[u][1]), cvtpk(qx[u][2], qx[u][3])};
    *(__attribute__((address_space(3))) u32x2*)((lds_ptr)smem + MISC + 1024 * w + (16 * (2 * u + (g4 >> 1)) + r16) * 16 + 8 * (g4 & 1)) = v;
  }

  const lds_cptr bK0 = L + 2048 * (r32 >> 3) + 64 * (r32 & 7) + 16 * ((0 + hi) ^ xq);
  const lds_cptr bK1 = L + 2048 * (r32 >> 3) + 64 * (r32 & 7) + 16 * ((2 + hi) ^ xq);
  const lds_cptr bV  = L + 256 * hi + 64 * qp + 32 * blk + 8 * p4;
  const lds_cptr bKx = L + 8192 * hf + 2048 * (r16 >> 3) + 64 * (r16 & 7) + 16 * (g4 ^ ((r16 >> 2) & 3));
  const lds_cptr bVx = L + 8192 * hf + 2048 * (g4 >> 1) + 256 * (g4 & 1) + 64 * qp + 8 * p4;
  const lds_cptr bQx = L + MISC + 16 * lane;
  f32x16 o[4] = {}; f32x4 ox[8] = {};
  float mc = -INFINITY, l = 0.f, mcx = -INFINITY, lx = 0.f;
#pragma unroll 1
  for (int t2 = 0; t2 < 4; ++t2) {
#pragma unroll
    for (int pr = 0; pr < 2; ++pr) {
      const int t = 2 * t2 + pr;
      const int ks_ = 2 * pr * SLOT, vs_ = ks_ + SLOT;
      if (t == 0) { WAIT_BAR(4); ldK(1, 2); ldV(1, 3); }
      else if (t < 7) { WAIT_BAR(8); ldV(t + 1, 2 * (pr ^ 1) + 1); }
      else { WAIT_BAR(4); }
      f32x16 s0 = {}, s1 = {};
#pragma unroll
      for (int s = 0; s < 8; ++s) {
        const lds_cptr kb = ((s & 1) ? bK1 : bK0) + ks_ + 512 * (s >> 1);
        s0 = MFMA32(ldsb128(kb), qf[s], s0);
        s1 = MFMA32(ldsb128(kb + 8192), qf[s], s1);
      }
      const bool act = (par == (unsigned)pr);
      bf16x8 px;
      if (act) {
        f32x4 e0 = {}, e1 = {};
#pragma unroll
        for (int s = 0; s < 4; ++s) {
          const bf16x8 qxf = ldsb128(bQx + 1024 * s);
          e0 = MFMA16(ldsb128(bKx + ks_ + 512 * s), qxf, e0);
          e1 = MFMA16(ldsb128(bKx + ks_ + 4096 + 512 * s), qxf, e1);
        }
        float rm = fmaxf(fmaxf(fmaxf(e0[0], e0[1]), fmaxf(e0[2], e0[3])), fmaxf(fmaxf(e1[0], e1[1]), fmaxf(e1[2], e1[3])));
        rm = swapmax32(swapmax16(rm)) * C2;
        if (__any(rm - mcx > THR)) {
          const float mn = fmaxf(mcx, rm), al = __builtin_amdgcn_exp2f(mcx - mn);
          mcx = mn; lx *= al;
#pragma unroll
          for (int d = 0; d < 8; ++d) ox[d] *= al;
        }
#pragma unroll
        for (int r = 0; r < 4; ++r) { e0[r] = __builtin_amdgcn_exp2f(fmaf(e0[r], C2, -mcx)); e1[r] = __builtin_amdgcn_exp2f(fmaf(e1[r], C2, -mcx)); }
        lx += ((e0[0] + e0[1]) + (e0[2] + e0[3])) + ((e1[0] + e1[1]) + (e1[2] + e1[3]));
        u32x4 pw = {cvtpk(e0[0], e0[1]), cvtpk(e0[2], e0[3]), cvtpk(e1[0], e1[1]), cvtpk(e1[2], e1[3])};
        px = __builtin_bit_cast(bf16x8, pw);
      }
      float rm = fmaxf(s0[0], s1[0]);
#pragma unroll
      for (int r = 1; r < 16; ++r) rm = fmaxf(rm, fmaxf(s0[r], s1[r]));
      rm = swapmax32(rm) * C2;
      if (__any(rm - mc > THR)) {
        const float mn = fmaxf(mc, rm), al = __builtin_amdgcn_exp2f(mc - mn);
        mc = mn; l *= al;
#pragma unroll
        for (int d = 0; d < 4; ++d) o[d] *= al;
      }
      float ls0 = 0.f, ls1 = 0.f;
#pragma unroll
      for (int r = 0; r < 16; ++r) {
        s0[r] = __builtin_amdgcn_exp2f(fmaf(s0[r], C2, -mc)); s1[r] = __builtin_amdgcn_exp2f(fmaf(s1[r], C2, -mc));
        ls0 += s0[r]; ls1 += s1[r];
      }
      l += ls0 + ls1;
      bf16x8 pf[4] = {pack8(s0, 0), pack8(s0, 1), pack8(s1, 0), pack8(s1, 1)};
      if (t < 6) { WAIT_BAR(8); ldK(t + 2, 2 * pr); }
      else if (t == 6) { WAIT_BAR(8); }
      else { WAIT_BAR(0); }
#pragma unroll
      for (int ks = 0; ks < 4; ++ks)
#pragma unroll
        for (int dt = 0; dt < 4; ++dt) o[dt] = MFMA32(ldstr2(bV + vs_ + 4096 * ks + 512 * dt, 2048), pf[ks], o[dt]);
      if (act) {
#pragma unroll
        for (int d = 0; d < 8; ++d) ox[d] = MFMA16(ldstr2(bVx + vs_ + 512 * (d >> 1) + 32 * (d & 1), 4096), px, ox[d]);
      }
    }
  }

  char* Ows = (char*)P.ws + (size_t)item * OITEM;
  {
    lx = swapadd32(swapadd16(lx));
#pragma unroll
    for (int d = 0; d < 8; ++d) *(__attribute__((address_space(3))) f32x4*)((lds_ptr)smem + 8192 * w + 1024 * d + 16 * lane) = ox[d];
    if (lane < 16) { f32x2 ml = {mcx, lx}; *(__attribute__((address_space(3))) f32x2*)((lds_ptr)smem + MISC + 4096 + 128 * w + 8 * lane) = ml; }
  }
  {
    const float inv = 1.0f / swapadd32(l);
#pragma unroll
    for (int dt = 0; dt < 4; ++dt) {
      o[dt] *= inv;
#pragma unroll
      for (int s = 0; s < 2; ++s) *(bf16x8*)(Ows + 8192 * w + 1024 * (2 * dt + s) + 16 * lane) = pack8(o[dt], s);
    }
  }
  asm volatile("s_waitcnt lgkmcnt(0)\n\ts_barrier" ::: "memory");
  {
    f32x2 ml[4];
#pragma unroll
    for (int i = 0; i < 4; ++i) ml[i] = *(const __attribute__((address_space(3))) f32x2*)(L + MISC + 4096 + 128 * i + 8 * r16);
    const float m = fmaxf(fmaxf(ml[0][0], ml[1][0]), fmaxf(ml[2][0], ml[3][0]));
    float f[4], lt = 0.f;
#pragma unroll
    for (int i = 0; i < 4; ++i) { f[i] = __builtin_amdgcn_exp2f(ml[i][0] - m); lt += f[i] * ml[i][1]; }
    const float inv = 1.0f / lt;
    f32x4 a0 = {}, a1 = {};
#pragma unroll
    for (int i = 0; i < 4; ++i) {
      a0 += *(const __attribute__((address_space(3))) f32x4*)(L + 8192 * i + 1024 * (2 * w) + 16 * lane) * f[i];
      a1 += *(const __attribute__((address_space(3))) f32x4*)(L + 8192 * i + 1024 * (2 * w + 1) + 16 * lane) * f[i];
    }
    a0 *= inv; a1 *= inv;
    u32x4 pw = {cvtpk(a0[0], a0[1]), cvtpk(a0[2], a0[3]), cvtpk(a1[0], a1[1]), cvtpk(a1[2], a1[3])};
    *(u32x4*)(Ows + 32768 + 1024 * w + 16 * lane) = pw;
  }
}

constexpr int LDS2 = 8 * SLOT;
DI void ldo(bf16x8& d, unsigned voff, rsrc_t srd, unsigned soff) {
  asm volatile("buffer_load_dwordx4 %0, %1, %2, %3 offen" : "=v"(d) : "v"(voff), "s"(srd), "s"(soff) : "memory");
}
__global__ __launch_bounds__(512, 2) void oproj_kernel(Ptrs P) {
  extern __shared__ __attribute__((aligned(16))) char smem[];
  const int tid = threadIdx.x, lane = tid & 63;
  const unsigned wid = rfl(tid >> 6), w = wid & 3, hg = wid >> 2;
  const int r32 = lane & 31, hi = lane >> 5, r16 = lane & 15, g4 = lane >> 4, qp = (lane & 15) >> 2, p4 = lane & 3, blk = (lane >> 4) & 1;
  const unsigned bid = blockIdx.x, xcd = bid & 7, g = xcd * 4 + ((bid >> 3) & 3), ns = bid >> 5;
  const unsigned br = g >> 4, b = g & 15, n0 = 64 * ns;
  const rsrc_t srdWo = mkrsrc((const char*)P.wo[br] + (size_t)hg * 2048 * 1024 + n0 * 2, 2048 * 1024 - n0 * 2);
  const rsrc_t srdO = mkrsrc((const char*)P.ws + (size_t)(g * NH + 16 * hg) * OITEM, 16 * OITEM);
  const unsigned lds0 = (unsigned)(uintptr_t)smem + hg * 4 * SLOT;
  const lds_cptr L = (lds_cptr)smem + hg * 4 * SLOT;
  const unsigned voW = 1024u * (32 * w + ((lane >> 2) & 7)) + 64 * hi + 16 * p4;
  const unsigned m0W = lds0 + 4096 * w;
  auto ldW = [&](int hh, int slot) { _Pragma("unroll") for (int i = 0; i < 4; ++i) glds<false>(m0W + slot * SLOT + 1024 * i, voW, srdWo, (unsigned)(hh * 128 * 1024 + i * 8 * 1024)); };
  const unsigned voO = 8192 * w + 16 * lane, voOx = 32768 + 16 * lane;
  bf16x8 of[2][8], oxf[2][4];
  auto ldO = [&](int hh, int set) {
    _Pragma("unroll") for (int i = 0; i < 8; ++i) ldo(of[set][i], voO, srdO, (unsigned)(hh * OITEM + 1024 * i));
    _Pragma("unroll") for (int i = 0; i < 4; ++i) ldo(oxf[set][i], voOx, srdO, (unsigned)(hh * OITEM + 1024 * i));
  };
  const lds_cptr bA  = L + 256 * hi + 64 * qp + 32 * blk + 8 * p4;
  const lds_cptr bAx = L + 1024 * (g4 >> 1) + 256 * (g4 & 1) + 64 * qp + 512 * (w >> 1) + 32 * (w & 1) + 8 * p4;
  f32x16 acc[2] = {}; f32x4 accx = {};
  asm volatile("s_nop 4" ::: "memory");
  ldO(0, 0); ldW(0, 0); ldW(1, 1); ldW(2, 2);
#pragma unroll
  for (int hh = 0; hh < 16; ++hh) {
    const int set = hh & 1, sl = (hh & 3) * SLOT;
#define OREGS(S) "+v"(of[S][0]), "+v"(of[S][1]), "+v"(of[S][2]), "+v"(of[S][3]), "+v"(of[S][4]), "+v"(of[S][5]), "+v"(of[S][6]), "+v"(of[S][7]), "+v"(oxf[S][0]), "+v"(oxf[S][1]), "+v"(oxf[S][2]), "+v"(oxf[S][3])
    if (hh == 0)       asm volatile("s_waitcnt vmcnt(8) lgkmcnt(0)\n\ts_barrier" : OREGS(set) :: "memory");
    else if (hh < 14)  asm volatile("s_waitcnt vmcnt(4) lgkmcnt(0)\n\ts_barrier" : OREGS(set) :: "memory");
    else               asm volatile("s_waitcnt vmcnt(0) lgkmcnt(0)\n\ts_barrier" : OREGS(set) :: "memory");
    if (hh + 1 < 16) ldO(hh + 1, set ^ 1);
    if (hh + 3 < 16) ldW(hh + 3, (hh + 3) & 3);
#pragma unroll
    for (int ks = 0; ks < 8; ++ks)
#pragma unroll
      for (int nt = 0; nt < 2; ++nt) acc[nt] = MFMA32(ldstr2(bA + sl + 2048 * ks + 512 * nt, 1024), of[set][ks], acc[nt]);
#pragma unroll
    for (int u = 0; u < 4; ++u) accx = MFMA16(ldstr2(bAx + sl + 4096 * u, 2048), oxf[set][u], accx);
  }
  asm volatile("s_waitcnt lgkmcnt(0)\n\ts_barrier" ::: "memory");
  lds_ptr red = (lds_ptr)smem + 9216 * w + 16 * lane;
  if (hg == 1) {
#pragma unroll
    for (int nt = 0; nt < 2; ++nt)
#pragma unroll
      for (int i = 0; i < 4; ++i) { f32x4 v = {acc[nt][4 * i], acc[nt][4 * i + 1], acc[nt][4 * i + 2], acc[nt][4 * i + 3]}; *(__attribute__((address_space(3))) f32x4*)(red + 1024 * (4 * nt + i)) = v; }
    *(__attribute__((address_space(3))) f32x4*)(red + 8192) = accx;
  }
  asm volatile("s_waitcnt lgkmcnt(0)\n\ts_barrier" ::: "memory");
  if (hg == 0) {
    char* outb = (char*)P.out + ((size_t)b * SQ * 1024 + br * 512 + n0) * 2;
#pragma unroll
    for (int nt = 0; nt < 2; ++nt) {
      unsigned pk[8];
#pragma unroll
      for (int i = 0; i < 4; ++i) {
        const f32x4 v = *(const __attribute__((address_space(3))) f32x4*)(red + 1024 * (4 * nt + i));
        pk[2 * i] = cvtpk(acc[nt][4 * i] + v[0], acc[nt][4 * i + 1] + v[1]); pk[2 * i + 1] = cvtpk(acc[nt][4 * i + 2] + v[2], acc[nt][4 * i + 3] + v[3]);
      }
#pragma unroll
      for (int i = 0; i < 4; i += 2) {
        auto r0 = __builtin_amdgcn_permlane32_swap(pk[2 * i], pk[2 * i + 2], false, false);
        auto r1 = __builtin_amdgcn_permlane32_swap(pk[2 * i + 1], pk[2 * i + 3], false, false);
        u32x4 v = {r0[0], r1[0], r0[1], r1[1]};
        *(u32x4*)(outb + (size_t)(32 * w + r32) * 2048 + (32 * nt + 8 * i + 8 * hi) * 2) = v;
      }
    }
    const f32x4 v = *(const __attribute__((address_space(3))) f32x4*)(red + 8192);
    u32x2 px = {cvtpk(accx[0] + v[0], accx[1] + v[1]), cvtpk(accx[2] + v[2], accx[3] + v[3])};
    *(u32x2*)(outb + (size_t)(128 + r16) * 2048 + (16 * w + 4 * g4) * 2) = px;
  }
}

extern "C" void kernel_launch(void* const* d_in, const int* in_sizes, int n_in, void* d_out, int out_size, void* d_ws, size_t ws_size, hipStream_t stream) {
  (void)in_sizes; (void)n_in; (void)out_size; (void)ws_size;
  Ptrs P;
  for (int br = 0; br < 2; ++br) { P.x[br] = d_in[5 * br]; P.k[br] = d_in[5 * br + 1]; P.v[br] = d_in[5 * br + 2]; P.wq[br] = d_in[5 * br + 3]; P.wo[br] = d_in[5 * br + 4]; }
  P.out = d_out; P.ws = d_ws;
  (void)hipFuncSetAttribute((const void*)attn_kernel, hipFuncAttributeMaxDynamicSharedMemorySize, LDS1);
  (void)hipFuncSetAttribute((const void*)oproj_kernel, hipFuncAttributeMaxDynamicSharedMemorySize, LDS2);
  hipLaunchKernelGGL(attn_kernel, dim3(1024), dim3(256), LDS1, stream, P);
  hipLaunchKernelGGL(oproj_kernel, dim3(256), dim3(512), LDS2, stream, P);
}
```

```cpp
#include <hip/hip_runtime.h>
#include <hip/hip_bf16.h>
#include <cstdint>
#include <type_traits>

typedef __bf16   bf16x8 __attribute__((ext_vector_type(8)));
typedef __bf16   bf16x4 __attribute__((ext_vector_type(4)));
typedef __bf16   bf16x2 __attribute__((ext_vector_type(2)));
typedef short    s16x4  __attribute__((ext_vector_type(4)));
typedef float    f32x16 __attribute__((ext_vector_type(16)));
typedef float    f32x4  __attribute__((ext_vector_type(4)));
typedef float    f32x2  __attribute__((ext_vector_type(2)));
typedef unsigned u32x4  __attribute__((ext_vector_type(4)));
typedef unsigned u32x2  __attribute__((ext_vector_type(2)));
typedef __attribute__((address_space(3))) char* lds_ptr;
typedef __attribute__((address_space(3))) const char* lds_cptr;
typedef __amdgpu_buffer_rsrc_t rsrc_t;

#define DI __device__ __forceinline__
#define MFMA32(a, b, c) __builtin_amdgcn_mfma_f32_32x32x16_bf16((a), (b), (c), 0, 0, 0)
#define MFMA16(a, b, c) __builtin_amdgcn_mfma_f32_16x16x32_bf16((a), (b), (c), 0, 0, 0)
#define WAIT_BAR(N) asm volatile("s_waitcnt vmcnt(" #N ") lgkmcnt(0)\n\ts_barrier" ::: "memory")
#define WAITV_BAR(N) asm volatile("s_waitcnt vmcnt(" #N ")\n\ts_barrier" ::: "memory")
#define SBAR() __builtin_amdgcn_sched_barrier(0)

constexpr int NB = 16, SQ = 144, DM = 512, NH = 32, SK = 512, DH = 128;
constexpr float C2 = 0.08838834764831845f * 1.4426950408889634f;
constexpr float THR = 8.0f;
constexpr int LDSB = 163840, QXO = 131072, XEO = 147456, STG = 49152;
constexpr size_t WS_CNT = 0, WS_O = 32768, WS_PART = WS_O + (size_t)1024 * 36 * 1024;
constexpr int PARTB = 72 * 1024;
constexpr int OITEM = 36 * 1024;

DI unsigned rfl(unsigned x) { return __builtin_amdgcn_readfirstlane(x); }
DI unsigned cvtpk(float lo, float hi) { f32x2 v = {lo, hi}; bf16x2 b = __builtin_convertvector(v, bf16x2); return __builtin_bit_cast(unsigned, b); }
DI rsrc_t mkrsrc(const void* p, unsigned bytes) { return __builtin_amdgcn_make_buffer_rsrc((void*)p, (short)0, (int)bytes, 0x00020000); }
template <bool NT> DI void glds(unsigned m0v, unsigned voff, rsrc_t srd, unsigned soff) {
  if constexpr (NT) asm volatile("s_mov_b32 m0, %0\n\ts_nop 0\n\tbuffer_load_dwordx4 %1, %2, %3 offen nt lds" :: "s"(m0v), "v"(voff), "s"(srd), "s"(soff) : "memory");
  else              asm volatile("s_mov_b32 m0, %0\n\ts_nop 0\n\tbuffer_load_dwordx4 %1, %2, %3 offen lds" :: "s"(m0v), "v"(voff), "s"(srd), "s"(soff) : "memory");
}
DI bf16x8 ldsb128(lds_cptr p) { return *(const __attribute__((address_space(3))) bf16x8*)p; }
DI bf16x4 ldstr(lds_cptr p) { return __builtin_bit_cast(bf16x4, __builtin_amdgcn_ds_read_tr16_b64_v4i16((__attribute__((address_space(3))) s16x4*)p)); }
DI bf16x8 ldstr2(lds_cptr p, int off2) { bf16x4 a = ldstr(p), b = ldstr(p + off2); return __builtin_shufflevector(a, b, 0, 1, 2, 3, 4, 5, 6, 7); }
DI bf16x8 pack8(const f32x16& x, int s) {
  u32x4 w = {cvtpk(x[8 * s + 0], x[8 * s + 1]), cvtpk(x[8 * s + 2], x[8 * s + 3]), cvtpk(x[8 * s + 4], x[8 * s + 5]), cvtpk(x[8 * s + 6], x[8 * s + 7])};
  return __builtin_bit_cast(bf16x8, w);
}
DI float swapmax32(float m) { auto r = __builtin_amdgcn_permlane32_swap(__float_as_uint(m), __float_as_uint(m), false, false); return fmaxf(__uint_as_float(r[0]), __uint_as_float(r[1])); }
DI float swapadd32(float m) { auto r = __builtin_amdgcn_permlane32_swap(__float_as_uint(m), __float_as_uint(m), false, false); return __uint_as_float(r[0]) + __uint_as_float(r[1]); }
DI float swapmax16(float m) { auto r = __builtin_amdgcn_permlane16_swap(__float_as_uint(m), __float_as_uint(m), false, false); return fmaxf(__uint_as_float(r[0]), __uint_as_float(r[1])); }
DI float swapadd16(float m) { auto r = __builtin_amdgcn_permlane16_swap(__float_as_uint(m), __float_as_uint(m), false, false); return __uint_as_float(r[0]) + __uint_as_float(r[1]); }

struct Ptrs { const void* x[2]; const void* k[2]; const void* v[2]; const void* wq[2]; const void* wo[2]; void* out; void* ws; };

#define PIN(x) asm volatile("" : "+v"(x))
#define MX3(a, b, c) __builtin_fmaxf(__builtin_fmaxf((a), (b)), (c))
#define STAMP(i) do {} while (0)
#define TLOG(v, i) do {} while (0)
DI void mma(f32x4& acc, bf16x8 a, bf16x8 b) { asm volatile("v_mfma_f32_16x16x32_bf16 %0, %1, %2, %0" : "+v"(acc) : "v"(a), "v"(b)); }
DI void mmc(f32x4& d, bf16x8 a, bf16x8 b, const f32x4& cin) { asm volatile("v_mfma_f32_16x16x32_bf16 %0, %1, %2, %3" : "=&v"(d) : "v"(a), "v"(b), "v"(cin)); }
DI void mm0(f32x4& d, bf16x8 a, bf16x8 b) { asm volatile("v_mfma_f32_16x16x32_bf16 %0, %1, %2, 0" : "=&v"(d) : "v"(a), "v"(b)); }
#define NOP2() asm volatile("s_nop 1")
#define NOP8() asm volatile("s_nop 7")
DI float ex2(float v) { return __builtin_amdgcn_exp2f(v); }
DI bf16x8 pack44(const f32x4& a, const f32x4& b) { u32x4 w = {cvtpk(a[0], a[1]), cvtpk(a[2], a[3]), cvtpk(b[0], b[1]), cvtpk(b[2], b[3])}; return __builtin_bit_cast(bf16x8, w); }
DI float rowmax4(float m) {
  { auto r = __builtin_amdgcn_permlane16_swap(__float_as_uint(m), __float_as_uint(m), false, false); m = MX3(__uint_as_float(r[0]), __uint_as_float(r[1]), m); }
  { auto r = __builtin_amdgcn_permlane32_swap(__float_as_uint(m), __float_as_uint(m), false, false); m = MX3(__uint_as_float(r[0]), __uint_as_float(r[1]), m); }
  return m;
}

struct St {
  bf16x8 qf[2][4];
  f32x4 o[2][8], ls[2];
  f32x4 ox[8]; float lx, mx;
  f32x4 s[2][2][2];
  f32x4 sx[2];
  f32x4 nm[2];
  u32x4 pw[2], pwx;
  bf16x8 kf[8], vf[8], qx[8], ones;
  float ma;
  lds_cptr bK, bV0, bV1, bQx;
};
DI void rdK(St& c, int g) { c.kf[g] = ldsb128(c.bK + 4096 * (g >> 2) + 512 * (g & 3)); }
DI void rdV(St& c, int d) { c.vf[d] = ldstr2(((d & 1) ? c.bV1 : c.bV0) + 512 * (d >> 1), 4096); }

template <int X, bool FIRST> DI void decide_main(St& c) {
  if (FIRST || __builtin_expect(__any(c.ma > THR), 0)) {
    SBAR(); NOP8(); SBAR();
#pragma unroll
    for (int a = 0; a < 2; ++a) {
      const f32x4 &u = c.s[X][0][a], &v = c.s[X][1][a];
      float m = rowmax4(MX3(MX3(MX3(u[0], u[1], u[2]), u[3], v[0]), MX3(v[1], v[2], v[3]), v[3]));
      if (!FIRST) m = fmaxf(m, 0.f);
      c.s[X][0][a] -= m; c.s[X][1][a] -= m; c.nm[a] -= m;
      if (!FIRST) {
        const float al = ex2(-m);
        c.ls[a] *= al;
#pragma unroll
        for (int d = 0; d < 8; ++d) c.o[a][d] *= al;
      }
    }
    SBAR(); NOP2(); SBAR();
  }
}
DI void decide_x(St& c) {
  const f32x4 &u = c.sx[0], &v = c.sx[1];
  const float lm = MX3(MX3(MX3(u[0], u[1], u[2]), u[3], v[0]), MX3(v[1], v[2], v[3]), v[3]);
  if (__builtin_expect(__any(lm - c.mx > THR), 0)) {
    SBAR(); NOP8(); SBAR();
    const float mn = fmaxf(rowmax4(lm), c.mx), al = ex2(c.mx - mn);
    c.mx = mn; c.lx *= al;
#pragma unroll
    for (int d = 0; d < 8; ++d) c.ox[d] *= al;
    SBAR(); NOP2(); SBAR();
  }
}

struct NoDma { DI void operator()(int) const {} };
template <int X, bool QK, bool XQ, bool EXP, bool XE, bool NV, class F = NoDma> DI void phaseA(St& c, F dma = F{}) {
  if (XQ) { c.qx[0] = ldsb128(c.bQx); c.qx[1] = ldsb128(c.bQx + 1024); }
#pragma unroll
  for (int g = 0; g < 8; ++g) {
    const int tau = g >> 2, s = g & 3;
    if (QK && g < 4) rdK(c, g + 4);
    if (XQ && g < 6) c.qx[g + 2] = ldsb128(c.bQx + 1024 * ((g + 2) & 3));
    if (NV && g >= 4) rdV(c, g - 4);
    dma(g);
    SBAR();
    if (QK) { if (s == 0) mmc(c.s[X ^ 1][tau][0], c.kf[g], c.qf[0][s], c.nm[0]); else mma(c.s[X ^ 1][tau][0], c.kf[g], c.qf[0][s]); }
    if (EXP) { f32x4& v = c.s[X][(g >> 1) & 1][g >> 2]; v[2 * (g & 1)] = ex2(v[2 * (g & 1)]); PIN(v); }
    SBAR();
    if (QK) { if (s == 0) mmc(c.s[X ^ 1][tau][1], c.kf[g], c.qf[1][s], c.nm[1]); else mma(c.s[X ^ 1][tau][1], c.kf[g], c.qf[1][s]); }
    if (EXP) { f32x4& v = c.s[X][(g >> 1) & 1][g >> 2]; v[2 * (g & 1) + 1] = ex2(v[2 * (g & 1) + 1]); PIN(v);
      if (g > 0) { const int p = g - 1; const f32x4& u = c.s[X][(p >> 1) & 1][p >> 2]; c.pw[p >> 2][p & 3] = cvtpk(u[2 * (p & 1)], u[2 * (p & 1) + 1]); PIN(c.pw[p >> 2]); } }
    SBAR();
    if (XQ) { if (s == 0) mm0(c.sx[tau], c.kf[g], c.qx[g]); else mma(c.sx[tau], c.kf[g], c.qx[g]); SBAR(); }
    if (XE) { f32x4& v = c.sx[g >> 2]; v[g & 3] = ex2(v[g & 3] - c.mx); c.lx += v[g & 3]; PIN(v); PIN(c.lx);
      if (g & 1) { c.pwx[g >> 1] = cvtpk(v[(g & 3) - 1], v[g & 3]); PIN(c.pwx); } SBAR(); }
  }
  if (EXP) { const f32x4& u = c.s[X][1][1]; c.pw[1][3] = cvtpk(u[2], u[3]); }
}
template <int X, bool MAX, bool XP, bool NK, class F = NoDma> DI void phaseB(St& c, F dma = F{}) {
  PIN(c.pw[1]); SBAR(); NOP2();
  const bf16x8 p0 = __builtin_bit_cast(bf16x8, c.pw[0]), p1 = __builtin_bit_cast(bf16x8, c.pw[1]);
#pragma unroll
  for (int g = 0; g < 8; ++g) {
    if (g < 4) rdV(c, g + 4);
    if (NK && g >= 4) rdK(c, g - 4);
    dma(g);
    SBAR();
    mma(c.o[0][g], c.vf[g], p0);
    if (MAX && g >= 2 && g < 6) {
      const f32x4& n = c.s[X ^ 1][(g - 2) >> 1][(g - 2) & 1];
      if (g == 2) c.ma = MX3(n[0], n[1], n[2]); else c.ma = MX3(c.ma, n[0], n[1]);
      PIN(c.ma);
    }
    SBAR();
    mma(c.o[1][g], c.vf[g], p1);
    if (MAX && g >= 2 && g < 6) {
      const f32x4& n = c.s[X ^ 1][(g - 2) >> 1][(g - 2) & 1];
      if (g == 2) c.ma = MX3(c.ma, n[3], n[3]); else c.ma = MX3(c.ma, n[2], n[3]);
      PIN(c.ma);
    }
    SBAR();
    if (XP) { mma(c.ox[g], c.vf[g], __builtin_bit_cast(bf16x8, c.pwx)); SBAR(); }
    if (g == 0) { mma(c.ls[0], c.ones, p0); SBAR(); }
    if (g == 1) { mma(c.ls[1], c.ones, p1); SBAR(); }
  }
}

typedef __attribute__((address_space(1))) unsigned gu32;
#define RLX_AGENT __ATOMIC_RELAXED, __HIP_MEMORY_SCOPE_AGENT
DI void ldo(bf16x8& d, unsigned voff, rsrc_t srd, unsigned soff) {
  asm volatile("buffer_load_dwordx4 %0, %1, %2, %3 offen sc1" : "=v"(d) : "v"(voff), "s"(srd), "s"(soff) : "memory");
}
DI void sto(bf16x8 v, unsigned voff, rsrc_t srd, unsigned off) { __builtin_amdgcn_raw_buffer_store_b128(__builtin_bit_cast(u32x4, v), srd, voff + off, 0, 16); }
#define XORV(r, k) asm volatile("v_xor_b32 %0, %1, %0" : "+v"(r) : "i"(k))
#define LDSW(T, p) (*(__attribute__((address_space(3))) T*)(p))
#define LDSR(T, p) (*(const __attribute__((address_space(3))) T*)(p))

DI unsigned lane_id_();
DI unsigned lane_id() { unsigned l; asm volatile("v_mbcnt_lo_u32_b32 %0, -1, 0\n\tv_mbcnt_hi_u32_b32 %0, -1, %0" : "=v"(l)); return l; }
DI unsigned opq(unsigned x) { asm volatile("" : "+s"(x)); return x; }
DI unsigned sel(unsigned a, unsigned b, unsigned long long m) { unsigned r; asm("v_cndmask_b32 %0, %1, %2, %3" : "=v"(r) : "v"(a), "v"(b), "s"(m)); return r; }
DI lds_cptr xor32(lds_cptr p) { return (lds_cptr)(uintptr_t)((unsigned)(uintptr_t)p ^ 32u); }
struct Id { unsigned w, g, hf, par, wq2, grp, jq, br, b, h0; };
DI Id mkid(unsigned wid, unsigned bid) {
  wid = opq(wid); bid = opq(bid);
  Id i; i.w = wid & 3; i.g = wid >> 2; i.hf = wid & 1; i.par = (wid >> 1) & 1; i.wq2 = wid >> 1;
  i.grp = 4 * (bid & 7) + ((bid >> 3) & 3); i.jq = bid >> 5; i.br = i.grp >> 4; i.b = i.grp & 15; i.h0 = 4 * i.jq; return i;
}
struct StageLd {
  rsrc_t srdX, srdW; unsigned voW, voX, m0, jq;
  DI void init(const Ptrs& P, unsigned wid, unsigned bid, unsigned lds0, unsigned rnd) {
    const Id i = mkid(wid, bid); const unsigned lane = lane_id(), lr = (lane >> 2) & 7, hi = lane >> 5, p4 = lane & 3;
    srdX = mkrsrc((const char*)P.x[i.br] + (size_t)i.b * SQ * DM * 2, SQ * DM * 2);
    srdW = mkrsrc((const char*)P.wq[i.br] + i.h0 * 256 + rnd * 512, DM * NH * DH * 2 - i.h0 * 256 - rnd * 512);
    voW = 8192u * (8 * i.hf + lr) + 16 * (8 * i.wq2 + 4 * hi + (p4 ^ (2 * i.hf)));
    voX = 1024u * (8 * i.hf + 32 * i.wq2 + lr) + 16 * (4 * hi + (p4 ^ (2 * i.hf)));
    m0 = lds0 + 1024 * i.wq2; jq = i.jq | (i.hf << 8);
  }
  DI void piece(unsigned step, int k) const {
    const unsigned hf = jq >> 8, ks = (step + jq) & 7, sb = STG * ((step + 2) % 3);
    if (k < 4) glds<false>(m0 + sb + 4096 * hf + 8192 * k, voW, srdW, ks * (64 * 8192) + k * (16 * 8192));
    else glds<false>(4 * m0 + sb + 32768 + 1024 * hf + 2048 * (k - 4), voX, srdX, ks * 128 + (k - 4) * (16 * 1024));
  }
  DI void operator()(unsigned step) const { _Pragma("unroll") for (int k = 0; k < 6; ++k) piece(step, k); }
  DI void touch(unsigned& dum, unsigned wid, unsigned grp) const {
    const unsigned lane = lane_id();
    asm volatile("buffer_load_dword %0, %1, %2, %3 offen sc1" : "+v"(dum) : "v"(8192u * (lane >> 2) + 128u * (lane & 3)), "s"(srdW), "s"(8192u * (128 * (grp & 3) + 16 * wid)) : "memory");
    asm volatile("buffer_load_dword %0, %1, %2, %3 offen sc1" : "+v"(dum) : "v"(1024u * (lane < 18 ? lane : 17)), "s"(srdX), "s"(1024u * 18 * wid + 128u * (jq & 7)) : "memory");
  }
};
DI unsigned uhead(const Id& i, unsigned n) { return 4 * (((n + 4 * (i.jq >> 2)) & 7) ^ (i.jq & 3)) + 2 * (i.jq >> 2) + i.g; }
DI unsigned woSlot(unsigned g, unsigned hs) { return 32768u * ((0x2013u >> (4 * (hs & 3))) & 3u) + 16384 * g; }
struct WoLd {
  rsrc_t srd; unsigned vo, m0; Id i;
  DI void init(const Ptrs& P, unsigned wid, unsigned bid, unsigned lds0) {
    i = mkid(wid, bid); const unsigned lane = lane_id(), lr = (lane >> 2) & 7, hi = lane >> 5, p4 = lane & 3, blk = (lane >> 4) & 1;
    srd = mkrsrc((const char*)P.wo[i.br] + (i.jq & 3) * 256, 4096 * 1024 - (i.jq & 3) * 256);
    vo = 1024u * (8 * i.par + lr) + 16 * (4 * (2 * i.hf + hi) + (p4 ^ (2 * blk)));
    m0 = lds0 + 2048 * i.par + 1024 * i.hf;
  }
  DI void piece(unsigned hs, int k) const {
    const unsigned so = hs < 16 ? (uhead(i, hs >> 1) * 128 + 64 * (hs & 1)) * 1024 : 0x40000000u;
    glds<false>(m0 + woSlot(i.g, hs) + 4096 * k, vo, srd, so + k * (16 * 1024));
  }
  DI void operator()(unsigned hs) const { _Pragma("unroll") for (int k = 0; k < 4; ++k) piece(hs, k); }
};
#define LANEVARS const int lane = lane_id(), hi = lane >> 5, r16 = lane & 15, g4 = lane >> 4, qp = (lane & 15) >> 2, p4 = lane & 3, blk = (lane >> 4) & 1, lr = (lane >> 2) & 7, \
                 rowoff = 64 * (r16 & 7) + 16 * (g4 ^ (2 * (r16 >> 3))); (void)hi; (void)qp; (void)p4; (void)blk; (void)lr; (void)rowoff
#define IDVARS const Id id_ = mkid(wid, bid); const unsigned w = id_.w, g = id_.g, hf = id_.hf, par = id_.par, wq2 = id_.wq2, grp = id_.grp, jq = id_.jq, br = id_.br, b = id_.b, h0 = id_.h0; \
               (void)w; (void)g; (void)hf; (void)par; (void)wq2; (void)grp; (void)jq; (void)br; (void)b; (void)h0
#define AINL __attribute__((always_inline))
template <int N> using IC = std::integral_constant<int, N>;
DI void wait_count(gu32* cnt, unsigned want) {
  unsigned spins = 0;
  while ((unsigned)rfl(__hip_atomic_load(cnt, RLX_AGENT)) < want && ++spins < (1u << 16)) __builtin_amdgcn_s_sleep(1);
}

__global__ __launch_bounds__(512, 2) void fused_kernel(Ptrs P) {
  extern __shared__ __attribute__((aligned(16))) char smem[];
  const unsigned wid = rfl(threadIdx.x >> 6), bid = blockIdx.x;
  STAMP(0);
  const unsigned lds0 = (unsigned)(uintptr_t)smem;
  const lds_cptr L = (lds_cptr)smem;
  asm volatile("s_nop 4" ::: "memory");
  unsigned tdum = 0;
  { StageLd st; st.init(P, wid, bid, lds0, 0);
    st(0);
    IDVARS; LANEVARS;
    const unsigned voE = 1024u * (128 + 8 * hf + lr) + 16 * (4 * hi + (p4 ^ (2 * hf))) + 256 * wq2;
    _Pragma("unroll") for (int i = 0; i < 2; ++i) glds<false>(lds0 + XEO + 8192 * hf + 2048 * wq2 + 1024 * i, voE, st.srdX, 128u * i);
    st(1); st(2);
    st.touch(tdum, wid, grp); }

#pragma unroll 1
  for (unsigned rnd = 0; rnd < 2; ++rnd) {
    St c;
    IDVARS;
    const rsrc_t srdK = mkrsrc((const char*)P.k[br] + (size_t)(b * NH + h0 + 2 * rnd + g) * SK * DH * 2, SK * DH * 2);
    const rsrc_t srdV = mkrsrc((const char*)P.v[br] + (size_t)(b * NH + h0 + 2 * rnd + g) * SK * DH * 2, SK * DH * 2);
    unsigned voK, voV;
    { LANEVARS; voK = 256u * (8 * par + lr) + 16 * (4 * (2 * hf + hi) + (p4 ^ (2 * par))); voV = 256u * (8 * par + lr) + 16 * (4 * (2 * hf + hi) + (p4 ^ (2 * blk))); }
    const unsigned m0T = lds0 + 16384 * g + 2048 * par + 1024 * hf;
    auto pcK = [&](unsigned t, int i) AINL { glds<true>(m0T + 65536 * ((t + 1) & 1) + 4096 * i, voK, srdK, t * 16384 + i * 4096); };
    auto pcV = [&](unsigned t, int i) AINL { glds<true>(m0T + 32768 + 65536 * ((t + 1) & 1) + 4096 * i, voV, srdV, t * 16384 + i * 4096); };
    {
      LANEVARS;
      StageLd st; st.init(P, wid, bid, lds0, rnd);
      lds_cptr bW[4];
#pragma unroll
      for (int j = 0; j < 4; ++j) bW[j] = L + 4096 * g4 + 2048 * g + 512 * ((j + w) & 3) + 64 * qp + 32 * (g4 & 1) + 8 * p4;
      const lds_cptr bX = L + 32768 + 1024 * (4 * w + (r16 >> 3)) + rowoff;
      const lds_cptr bE = L + XEO + 8192 * (r16 >> 3) + rowoff;
      f32x4 qa[2][8] = {}, qx[2] = {};
#pragma unroll
      for (int d = 0; d < 8; ++d) { PIN(qa[0][d]); PIN(qa[1][d]); }
      PIN(qx[0]); PIN(qx[1]);
      constexpr int PD = 6;
      bf16x8 a[16], xb[2][2], xe[2];
      lds_cptr pW0[4], pW1[4], pX, pE;
      auto setp = [&](unsigned step) AINL {
        const unsigned sb = STG * ((step + 2) % 3);
#pragma unroll
        for (int j = 0; j < 4; ++j) { pW0[j] = bW[j] + sb; pW1[j] = xor32(pW0[j]); }
        pX = bX + sb; pE = bE + 1024 * ((step + jq) & 7);
      };
      auto rdA = [&](int m) AINL {
        if ((m & 7) == 0) { xb[m >> 3][0] = ldsb128(pX + 512 * (m >> 3)); xb[m >> 3][1] = ldsb128(pX + 2048 + 512 * (m >> 3)); xe[m >> 3] = ldsb128(pE + 512 * (m >> 3)); }
        a[m] = ldstr2(((m & 1) ? pW1[(m & 7) >> 1] : pW0[(m & 7) >> 1]) + 16384 * (m >> 3), 256);
      };
      unsigned tl = 0; (void)tl;
      auto qstep = [&](unsigned step, auto KINDC) AINL {
        constexpr int KIND = decltype(KINDC)::value;
        TLOG(tl, step);
#pragma unroll
        for (int m = 0; m < 16; ++m) {
          if (m == 16 - PD) {
            if (KIND == 0) { if (rnd == 1) { WAITV_BAR(15); } else { WAITV_BAR(8); } } else if (KIND == 1) { if (rnd == 1) { WAITV_BAR(9); } else { WAITV_BAR(2); } }
            else if (KIND == 7) { WAITV_BAR(4); } else { WAITV_BAR(0); }
            if (KIND != 7) setp(step + 1);
          }
          if (m + PD < 16) rdA(m + PD); else if (KIND != 7) rdA(m + PD - 16);
          if (m >= 16 - PD) { if (KIND == 1 || KIND == 2) st.piece(step + 2, m - (16 - PD)); else if (KIND == 6 && m < 20 - PD) pcK(0, m - (16 - PD)); else if (KIND == 7 && m < 20 - PD) pcV(0, m - (16 - PD)); }
          SBAR();
          mma(qa[0][m & 7], a[m], xb[m >> 3][0]);
          mma(qa[1][m & 7], a[m], xb[m >> 3][1]);
          if ((m & 7) < 2) mma(qx[m & 1], a[m], xe[m >> 3]);
          SBAR();
        }
      };
      setp(0);
      if (rnd == 1) { WAIT_BAR(21); } else { WAIT_BAR(14); }
#pragma unroll
      for (int m = 0; m < PD; ++m) rdA(m);
      SBAR();
      qstep(0, IC<0>{}); qstep(1, IC<1>{});
#pragma unroll 1
      for (unsigned step = 2; step < 6; ++step) qstep(step, IC<2>{});
      PIN(tdum);
      qstep(6, IC<6>{}); qstep(7, IC<7>{});
      TLOG(tl, 8);
      SBAR(); NOP8(); SBAR();
      unsigned qr[2][4][4];
#pragma unroll
      for (int a = 0; a < 2; ++a)
#pragma unroll
        for (int s = 0; s < 4; ++s) {
          const f32x4 u = qa[a][2 * s] * C2, v = qa[a][2 * s + 1] * C2;
          unsigned A0 = cvtpk(u[0], u[1]), A1 = cvtpk(u[2], u[3]), B0 = cvtpk(v[0], v[1]), B1 = cvtpk(v[2], v[3]);
          auto r0 = __builtin_amdgcn_permlane32_swap(A0, B0, false, false); auto r1 = __builtin_amdgcn_permlane32_swap(A1, B1, false, false);
          auto t0 = __builtin_amdgcn_permlane16_swap(r0[0], r0[1], false, false); auto t1 = __builtin_amdgcn_permlane16_swap(r1[0], r1[1], false, false);
          qr[a][s][0] = t0[0]; qr[a][s][1] = t1[0]; qr[a][s][2] = t0[1]; qr[a][s][3] = t1[1];
        }
      const unsigned long long m1 = (w & 1) ? ~0ull : 0ull, m2 = (w & 2) ? ~0ull : 0ull;
#pragma unroll
      for (int a = 0; a < 2; ++a) {
        unsigned t[4][4]; u32x4 q[4];
#pragma unroll
        for (int s = 0; s < 4; ++s)
#pragma unroll
          for (int e = 0; e < 4; ++e) t[s][e] = sel(qr[a][s][e], qr[a][(s + 3) & 3][e], m1);
#pragma unroll
        for (int s = 0; s < 4; ++s) {
#pragma unroll
          for (int e = 0; e < 4; ++e) q[s][e] = sel(t[s][e], t[(s + 2) & 3][e], m2);
          c.qf[a][s] = __builtin_bit_cast(bf16x8, q[s]);
        }
      }
#pragma unroll
      for (int u = 0; u < 2; ++u) {
        const f32x4 v = qx[u] * C2;
        u32x2 q = {cvtpk(v[0], v[1]), cvtpk(v[2], v[3])};
        LDSW(u32x2, (lds_ptr)smem + QXO + 4096 * g + 1024 * w + (16 * (2 * u + (g4 >> 1)) + r16) * 16 + 8 * (g4 & 1)) = q;
      }
    }
    STAMP(1 + 4 * rnd);

#define TOGK(k) XORV(c.bK, k)
#define TOGV(k) do { XORV(c.bV0, k); XORV(c.bV1, k); } while (0)
#define TILE(XC, XN, GLDS_STMT, LASTT) do { \
      WAIT_BAR(0); \
      _Pragma("unroll") for (int m = 0; m < 4; ++m) rdV(c, m); \
      SBAR(); \
      TOGK(0x12000); \
      phaseB<0, true, XC, !LASTT>(c, [&](int q) AINL { GLDS_STMT; }); decide_main<1, false>(c); \
      TOGV(0x2000); \
      phaseA<1, !LASTT, XN && !LASTT, true, false, true>(c); \
      TOGK(0x2000); \
      phaseB<1, !LASTT, false, !LASTT>(c); \
      TOGV(0x12000); \
      if (!LASTT) { decide_main<0, false>(c); if (XN) decide_x(c); phaseA<0, true, false, true, XN, false>(c); } \
    } while (0)
    auto attention = [&](auto PARC) AINL {
      constexpr bool XE = decltype(PARC)::value == 0, XO = !XE;
      { LANEVARS;
        c.bK  = L + 65536 + 16384 * g + 8192 * hf + 2048 * (r16 >> 3) + rowoff;
        c.bV0 = L + 65536 + 32768 + 16384 * g + 8192 * hf + 2048 * (g4 >> 1) + 256 * (g4 & 1) + 64 * qp + 32 * (g4 & 1) + 8 * p4;
        c.bV1 = xor32(c.bV0);
        c.bQx = L + QXO + 4096 * g + 16 * lane; }
#pragma unroll
      for (int d = 0; d < 8; ++d) { c.o[0][d] = f32x4{}; c.o[1][d] = f32x4{}; c.ox[d] = f32x4{}; }
      c.ls[0] = c.ls[1] = c.nm[0] = c.nm[1] = f32x4{};
#pragma unroll
      for (int d = 0; d < 8; ++d) { PIN(c.o[0][d]); PIN(c.o[1][d]); PIN(c.ox[d]); }
      PIN(c.ls[0]); PIN(c.ls[1]); PIN(c.nm[0]); PIN(c.nm[1]);
      c.mx = -INFINITY; c.lx = 0.f;
      { u32x4 one = {0x3F803F80u, 0x3F803F80u, 0x3F803F80u, 0x3F803F80u}; asm volatile("" : "+v"(one)); c.ones = __builtin_bit_cast(bf16x8, one); }
      WAIT_BAR(4);
#pragma unroll
      for (int s = 0; s < 4; ++s) rdK(c, s);
      SBAR();
      phaseA<1, true, XE, false, false, false>(c, [&](int q) AINL { if (q < 4) pcK(1, q); });
      TOGK(0x2000);
#pragma unroll
      for (int s = 0; s < 4; ++s) rdK(c, s);
      decide_main<0, true>(c); if (XE) decide_x(c);
      phaseA<0, true, false, true, XE, false>(c);
      STAMP(2 + 4 * rnd);
#pragma unroll 1
      for (unsigned t = 0; t < 6; t += 2) {
        TILE(XE, XO, { if (q < 4) pcV(t + 1, q); else pcK(t + 2, q - 4); }, false);
        TILE(XO, XE, { if (q < 4) pcV(t + 2, q); else pcK(t + 3, q - 4); }, false);
      }
      TILE(XE, XO, { if (q < 4) pcV(7, q); }, false);
      TILE(XO, XE, { if (q == 1) { if (rnd == 0) { StageLd st; st.init(P, wid, bid, lds0, 1); st(0); } else { WoLd wo; wo.init(P, wid, bid, lds0); wo(0); } } }, true);
      SBAR(); NOP8(); SBAR();
      STAMP(3 + 4 * rnd);
      LANEVARS;
      const lds_ptr scr = (lds_ptr)smem + 65536 * g;
      c.lx = swapadd32(swapadd16(c.lx));
#pragma unroll
      for (int d = 0; d < 8; ++d) LDSW(f32x4, scr + 8192 * w + 1024 * d + 16 * lane) = c.ox[d];
      const f32x4 own0 = LDSR(f32x4, scr + 10240 * w + 16 * lane), own1 = LDSR(f32x4, scr + 10240 * w + 1024 + 16 * lane);
      asm volatile("" ::: "memory");
      { f32x2 ml = {c.mx, c.lx}; LDSW(f32x2, scr + 10240 * w + 8 * lane) = ml; }
      bf16x8 ob[9];
#pragma unroll
      for (int a = 0; a < 2; ++a) {
        const float inv = __builtin_amdgcn_rcpf(c.ls[a][0]);
#pragma unroll
        for (int u = 0; u < 4; ++u) ob[4 * a + u] = pack44(c.o[a][2 * u] * inv, c.o[a][2 * u + 1] * inv);
      }
      asm volatile("s_waitcnt lgkmcnt(0)\n\ts_barrier" ::: "memory");
      {
        f32x2 ml[4];
#pragma unroll
        for (int i = 0; i < 4; ++i) ml[i] = LDSR(f32x2, scr + 10240 * i + 8 * r16);
        const float m = fmaxf(fmaxf(ml[0][0], ml[1][0]), fmaxf(ml[2][0], ml[3][0]));
        float f[4], lt = 0.f;
#pragma unroll
        for (int i = 0; i < 4; ++i) { f[i] = ex2(ml[i][0] - m); lt += f[i] * ml[i][1]; }
        const float inv = __builtin_amdgcn_rcpf(lt);
        f32x4 a0 = {}, a1 = {};
#pragma unroll
        for (int i = 0; i < 4; ++i) {
          f32x4 t0 = LDSR(f32x4, scr + 8192 * i + 2048 * w + 16 * lane), t1 = LDSR(f32x4, scr + 8192 * i + 2048 * w + 1024 + 16 * lane);
          if (w == (unsigned)i) { t0 = own0; t1 = own1; }
          a0 += t0 * f[i]; a1 += t1 * f[i];
        }
        ob[8] = pack44(a0 * inv, a1 * inv);
      }
      asm volatile("s_waitcnt lgkmcnt(0)\n\ts_barrier" ::: "memory");
      if (rnd == 0) { StageLd st; st.init(P, wid, bid, lds0, 1); st(1); st(2); } else { WoLd wo; wo.init(P, wid, bid, lds0); wo(1); wo(2); }
      const rsrc_t srdO = mkrsrc((const char*)P.ws + WS_O + (size_t)(opq(grp) * NH + h0 + 2 * rnd + g) * OITEM, OITEM);
#pragma unroll
      for (int i = 0; i < 8; ++i) sto(ob[i], 8192 * w + 16 * lane, srdO, 1024 * i);
      sto(ob[8], 32768 + 1024 * w + 16 * lane, srdO, 0);
    };
    if (par == 0) attention(IC<0>{}); else attention(IC<1>{});
    STAMP(4 + 4 * rnd);
    if (rnd == 0) {
    }
  }

  IDVARS; LANEVARS;
  gu32* cnt = (gu32*)((char*)P.ws + WS_CNT) + grp * 256;
  const unsigned typ = jq >> 2, ns = jq & 3;
  {
    asm volatile("s_waitcnt vmcnt(0) lgkmcnt(0)\n\ts_barrier" ::: "memory");
    if (wid == 0) {
      if (lane == 0) __hip_atomic_fetch_add(cnt, 1u, RLX_AGENT);
      wait_count(cnt, 8u);
      __builtin_amdgcn_fence(__ATOMIC_ACQUIRE, "agent");
      asm volatile("s_waitcnt vmcnt(0)" ::: "memory");
    }
    asm volatile("s_barrier" ::: "memory");
  }
  STAMP(9);

  {
    f32x4 acc[2][8] = {}, accx[8] = {};
    auto unit = [&](auto PARC) AINL {
      constexpr int XV = decltype(PARC)::value;
      WoLd wo; wo.init(P, wid, bid, lds0);
      const rsrc_t srdO = mkrsrc((const char*)P.ws + WS_O + (size_t)grp * NH * OITEM, NH * OITEM);
      const unsigned voO0 = 8192 * w + 1024 * hf + 16 * lane, voO1 = voO0 ^ 1024u, voOx = 32768 + 1024 * w + 16 * lane;
      bf16x8 of[4][5];
      auto pcO = [&](unsigned hs, auto SETC, int k) AINL {
        constexpr int set = decltype(SETC)::value;
        const unsigned so = hs < 16 ? uhead(id_, hs >> 1) * OITEM : 0x40000000u;
        if (k < 4) ldo(of[set][k], (k & 2) ? voO1 : voO0, srdO, so + 2048 * (set & 1) + 4096 * (k & 1));
        else if ((set & 1) == XV) ldo(of[set][4], voOx, srdO, so);
      };
      auto ldO = [&](unsigned hs, auto SETC) AINL { _Pragma("unroll") for (int k = 0; k < 5; ++k) pcO(hs, SETC, k); };
      const lds_cptr bA0 = L + 8192 * hf + 2048 * (g4 >> 1) + 256 * (g4 & 1) + 64 * qp + 32 * (g4 & 1) + 8 * p4;
#pragma unroll
      for (int d = 0; d < 8; ++d) { PIN(acc[0][d]); PIN(acc[1][d]); PIN(accx[d]); }
      ldO(0, IC<0>{}); ldO(1, IC<1>{}); ldO(2, IC<2>{});
      constexpr int PD = 6;
      bf16x8 a[16];
      lds_cptr pA0, pA1, pB0, pB1;
      auto setp = [&](int set) AINL { pA0 = bA0 + woSlot(g, set); pA1 = xor32(pA0); pB0 = (lds_cptr)(uintptr_t)((unsigned)(uintptr_t)pA0 ^ 8192u); pB1 = xor32(pB0); };
      auto rdA = [&](int m) AINL { a[m] = ldstr2(((m >> 3) ? ((m & 1) ? pB1 : pB0) : ((m & 1) ? pA1 : pA0)) + 512 * ((m & 7) >> 1), 4096); };
#define OWAIT(N, S) do { if (((S) & 1) == XV) asm volatile("s_waitcnt vmcnt(" #N ")\n\ts_barrier" : "+v"(of[S][0]), "+v"(of[S][1]), "+v"(of[S][2]), "+v"(of[S][3]), "+v"(of[S][4]) :: "memory"); \
                         else asm volatile("s_waitcnt vmcnt(" #N ")\n\ts_barrier" : "+v"(of[S][0]), "+v"(of[S][1]), "+v"(of[S][2]), "+v"(of[S][3]) :: "memory"); } while (0)
      unsigned tl = 0; (void)tl;
      auto ustep = [&](unsigned hs, auto SETC) AINL {
        constexpr int set = decltype(SETC)::value, nset = (set + 1) & 3;
        TLOG(tl, hs);
#pragma unroll
        for (int m = 0; m < 16; ++m) {
          if (m == 16 - PD) { if (set == 0) OWAIT(9, nset); else OWAIT(13, nset); setp(nset); }
          rdA((m + PD) & 15);
          if (m < 5) pcO(hs + 3, IC<(set + 3) & 3>{}, m); else if (m >= 16 - PD && m < 20 - PD) wo.piece(hs + 3, m - (16 - PD));
          SBAR();
          mma(acc[0][m & 7], a[m], of[set][2 * (m >> 3)]);
          mma(acc[1][m & 7], a[m], of[set][2 * (m >> 3) + 1]);
          if ((set & 1) == XV && m < 8) mma(accx[m], a[m], of[set][4]);
          SBAR();
        }
      };
      setp(0);
#pragma unroll
      for (int m = 0; m < PD; ++m) rdA(m);
      OWAIT(9, 0);
#pragma unroll 1
      for (unsigned hs = 0; hs < 16; hs += 4) { ustep(hs, IC<0>{}); ustep(hs + 1, IC<1>{}); ustep(hs + 2, IC<2>{}); ustep(hs + 3, IC<3>{}); }
      TLOG(tl, 16);
      asm volatile("s_waitcnt vmcnt(0)" : "+v"(of[0][0]), "+v"(of[0][1]), "+v"(of[0][2]), "+v"(of[0][3]), "+v"(of[1][0]), "+v"(of[1][1]), "+v"(of[1][2]), "+v"(of[1][3]),
                                           "+v"(of[2][0]), "+v"(of[2][1]), "+v"(of[2][2]), "+v"(of[2][3]), "+v"(of[XV][4]), "+v"(of[XV == 0 ? 2 : 1][4]) :: "memory");
      SBAR(); NOP8(); SBAR();
    };
    if (par == 0) unit(IC<0>{}); else unit(IC<1>{});
    STAMP(10);
    asm volatile("s_waitcnt lgkmcnt(0)\n\ts_barrier" ::: "memory");
    const lds_ptr red = (lds_ptr)smem + 16384 * w + 16 * lane, redx = (lds_ptr)smem + 65536 + 16 * lane;
    if (g == 1) {
#pragma unroll
      for (int i = 0; i < 16; ++i) LDSW(f32x4, red + 1024 * i) = acc[i >> 3][i & 7];
    }
#pragma unroll
    for (int i = 0; i < 8; ++i) LDSW(f32x4, redx + 8192 * wid + 1024 * i) = accx[i];
    asm volatile("s_waitcnt lgkmcnt(0)\n\ts_barrier" ::: "memory");
    f32x4 vx = {};
#pragma unroll
    for (int i = 0; i < 8; ++i) vx += LDSR(f32x4, redx + 8192 * i + 1024 * wid);
    if (g == 0) {
#pragma unroll
      for (int i = 0; i < 16; ++i) acc[i >> 3][i & 7] += LDSR(f32x4, red + 1024 * i);
    }
    const rsrc_t srdP = mkrsrc((const char*)P.ws + WS_PART + (size_t)(grp * 4 + ns) * PARTB, PARTB);
    if (typ == 0) {
      if (g == 0) {
#pragma unroll
        for (int i = 0; i < 16; ++i) __builtin_amdgcn_raw_buffer_store_b128(__builtin_bit_cast(u32x4, acc[i >> 3][i & 7]), srdP, 16384 * w + 16 * lane + 1024 * i, 0, 16);
      }
      __builtin_amdgcn_raw_buffer_store_b128(__builtin_bit_cast(u32x4, vx), srdP, 65536 + 1024 * wid + 16 * lane, 0, 16);
      asm volatile("s_waitcnt vmcnt(0)\n\ts_barrier" ::: "memory");
      if (wid == 0 && lane == 0) __hip_atomic_store(cnt + 32 * (1 + ns), 1u, RLX_AGENT);
    } else {
      if (wid == 0) {
        wait_count(cnt + 32 * (1 + ns), 1u);
        __builtin_amdgcn_fence(__ATOMIC_ACQUIRE, "agent");
        asm volatile("s_waitcnt vmcnt(0)" ::: "memory");
      }
      asm volatile("s_waitcnt lgkmcnt(0)\n\ts_barrier" ::: "memory");
      const lds_ptr tile = (lds_ptr)smem;
      vx += __builtin_bit_cast(f32x4, __builtin_amdgcn_raw_buffer_load_b128(srdP, 65536 + 1024 * wid + 16 * lane, 0, 16));
      if (g == 0) {
#pragma unroll
        for (int i = 0; i < 16; ++i) {
          const f32x4 v = acc[i >> 3][i & 7] + __builtin_bit_cast(f32x4, __builtin_amdgcn_raw_buffer_load_b128(srdP, 16384 * w + 16 * lane + 1024 * i, 0, 16));
          u32x2 px = {cvtpk(v[0], v[1]), cvtpk(v[2], v[3])};
          LDSW(u32x2, tile + (32 * w + 16 * (i >> 3) + r16) * 272 + (16 * (i & 7) + 4 * g4) * 2) = px;
        }
      }
      { u32x2 px = {cvtpk(vx[0], vx[1]), cvtpk(vx[2], vx[3])};
        LDSW(u32x2, tile + (128 + r16) * 272 + (16 * wid + 4 * g4) * 2) = px; }
      asm volatile("s_waitcnt lgkmcnt(0)\n\ts_barrier" ::: "memory");
      char* outb = (char*)P.out + ((size_t)b * SQ * 1024 + br * 512 + 128 * ns) * 2;
#pragma unroll
      for (int i = 0; i < 5; ++i) {
        const int row = 32 * i + 4 * wid + g4;
        if (row < SQ) *(u32x4*)(outb + (size_t)row * 2048 + 16 * r16) = LDSR(u32x4, tile + row * 272 + 16 * r16);
      }
    }
  }
  STAMP(11);
}

extern "C" void kernel_launch(void* const* d_in, const int* in_sizes, int n_in, void* d_out, int out_size, void* d_ws, size_t ws_size, hipStream_t stream) {
  (void)in_sizes; (void)n_in; (void)out_size; (void)ws_size;
  Ptrs P;
  for (int br = 0; br < 2; ++br) { P.x[br] = d_in[5 * br]; P.k[br] = d_in[5 * br + 1]; P.v[br] = d_in[5 * br + 2]; P.wq[br] = d_in[5 * br + 3]; P.wo[br] = d_in[5 * br + 4]; }
  P.out = d_out; P.ws = d_ws;
  (void)hipFuncSetAttribute((const void*)fused_kernel, hipFuncAttributeMaxDynamicSharedMemorySize, LDSB);
  (void)hipMemsetAsync((char*)d_ws + WS_CNT, 0, 32768, stream);
  hipLaunchKernelGGL(fused_kernel, dim3(256), dim3(512), LDSB, stream, P);
}
```

```cpp
#include <hip/hip_runtime.h>
#include <hip/hip_bf16.h>
#include <cstdint>
#include <type_traits>
#define TOUCH 1

typedef __bf16   bf16x8 __attribute__((ext_vector_type(8)));
typedef __bf16   bf16x4 __attribute__((ext_vector_type(4)));
typedef __bf16   bf16x2 __attribute__((ext_vector_type(2)));
typedef short    s16x4  __attribute__((ext_vector_type(4)));
typedef float    f32x16 __attribute__((ext_vector_type(16)));
typedef float    f32x4  __attribute__((ext_vector_type(4)));
typedef float    f32x2  __attribute__((ext_vector_type(2)));
typedef unsigned u32x4  __attribute__((ext_vector_type(4)));
typedef unsigned u32x2  __attribute__((ext_vector_type(2)));
typedef __attribute__((address_space(3))) char* lds_ptr;
typedef __attribute__((address_space(3))) const char* lds_cptr;
typedef __amdgpu_buffer_rsrc_t rsrc_t;

#define DI __device__ __forceinline__
#define MFMA32(a, b, c) __builtin_amdgcn_mfma_f32_32x32x16_bf16((a), (b), (c), 0, 0, 0)
#define MFMA16(a, b, c) __builtin_amdgcn_mfma_f32_16x16x32_bf16((a), (b), (c), 0, 0, 0)
#define WAIT_BAR(N) asm volatile("s_waitcnt vmcnt(" #N ") lgkmcnt(0)\n\ts_barrier" ::: "memory")
#define WAITV_BAR(N) asm volatile("s_waitcnt vmcnt(" #N ")\n\ts_barrier" ::: "memory")
#define SBAR() __builtin_amdgcn_sched_barrier(0)

constexpr int NB = 16, SQ = 144, DM = 512, NH = 32, SK = 512, DH = 128;
constexpr float C2 = 0.08838834764831845f * 1.4426950408889634f;
constexpr float THR = 8.0f;
constexpr int LDSB = 163840, QXO = 131072, XEO = 147456, STG = 49152;
constexpr size_t WS_CNT = 0, WS_O = 32768, WS_PART = WS_O + (size_t)1024 * 36 * 1024;
constexpr int PARTB = 72 * 1024;
constexpr int OITEM = 36 * 1024;

DI unsigned rfl(unsigned x) { return __builtin_amdgcn_readfirstlane(x); }
DI unsigned cvtpk(float lo, float hi) { f32x2 v = {lo, hi}; bf16x2 b = __builtin_convertvector(v, bf16x2); return __builtin_bit_cast(unsigned, b); }
DI rsrc_t mkrsrc(const void* p, unsigned bytes) { return __builtin_amdgcn_make_buffer_rsrc((void*)p, (short)0, (int)bytes, 0x00020000); }
template <bool NT> DI void glds(unsigned m0v, unsigned voff, rsrc_t srd, unsigned soff) {
  if constexpr (NT) asm volatile("s_mov_b32 m0, %0\n\ts_nop 0\n\tbuffer_load_dwordx4 %1, %2, %3 offen nt lds" :: "s"(m0v), "v"(voff), "s"(srd), "s"(soff) : "memory");
  else              asm volatile("s_mov_b32 m0, %0\n\ts_nop 0\n\tbuffer_load_dwordx4 %1, %2, %3 offen lds" :: "s"(m0v), "v"(voff), "s"(srd), "s"(soff) : "memory");
}
DI bf16x8 ldsb128(lds_cptr p) { return *(const __attribute__((address_space(3))) bf16x8*)p; }
DI bf16x4 ldstr(lds_cptr p) { return __builtin_bit_cast(bf16x4, __builtin_amdgcn_ds_read_tr16_b64_v4i16((__attribute__((address_space(3))) s16x4*)p)); }
DI bf16x8 ldstr2(lds_cptr p, int off2) { bf16x4 a = ldstr(p), b = ldstr(p + off2); return __builtin_shufflevector(a, b, 0, 1, 2, 3, 4, 5, 6, 7); }
DI bf16x8 pack8(const f32x16& x, int s) {
  u32x4 w = {cvtpk(x[8 * s + 0], x[8 * s + 1]), cvtpk(x[8 * s + 2], x[8 * s + 3]), cvtpk(x[8 * s + 4], x[8 * s + 5]), cvtpk(x[8 * s + 6], x[8 * s + 7])};
  return __builtin_bit_cast(bf16x8, w);
}
DI float swapmax32(float m) { auto r = __builtin_amdgcn_permlane32_swap(__float_as_uint(m), __float_as_uint(m), false, false); return fmaxf(__uint_as_float(r[0]), __uint_as_float(r[1])); }
DI float swapadd32(float m) { auto r = __builtin_amdgcn_permlane32_swap(__float_as_uint(m), __float_as_uint(m), false, false); return __uint_as_float(r[0]) + __uint_as_float(r[1]); }
DI float swapmax16(float m) { auto r = __builtin_amdgcn_permlane16_swap(__float_as_uint(m), __float_as_uint(m), false, false); return fmaxf(__uint_as_float(r[0]), __uint_as_float(r[1])); }
DI float swapadd16(float m) { auto r = __builtin_amdgcn_permlane16_swap(__float_as_uint(m), __float_as_uint(m), false, false); return __uint_as_float(r[0]) + __uint_as_float(r[1]); }

struct Ptrs { const void* x[2]; const void* k[2]; const void* v[2]; const void* wq[2]; const void* wo[2]; void* out; void* ws; };

#define PIN(x) asm volatile("" : "+v"(x))
#define MX3(a, b, c) __builtin_fmaxf(__builtin_fmaxf((a), (b)), (c))
#define STAMP(i) do {} while (0)
#define TLOG(v, i) do {} while (0)
DI void mma(f32x4& acc, bf16x8 a, bf16x8 b) { asm volatile("v_mfma_f32_16x16x32_bf16 %0, %1, %2, %0" : "+v"(acc) : "v"(a), "v"(b)); }
DI void mmc(f32x4& d, bf16x8 a, bf16x8 b, const f32x4& cin) { asm volatile("v_mfma_f32_16x16x32_bf16 %0, %1, %2, %3" : "=&v"(d) : "v"(a), "v"(b), "v"(cin)); }
DI void mm0(f32x4& d, bf16x8 a, bf16x8 b) { asm volatile("v_mfma_f32_16x16x32_bf16 %0, %1, %2, 0" : "=&v"(d) : "v"(a), "v"(b)); }
#define NOP2() asm volatile("s_nop 1")
#define NOP8() asm volatile("s_nop 7")
DI float ex2(float v) { return __builtin_amdgcn_exp2f(v); }
DI bf16x8 pack44(const f32x4& a, const f32x4& b) { u32x4 w = {cvtpk(a[0], a[1]), cvtpk(a[2], a[3]), cvtpk(b[0], b[1]), cvtpk(b[2], b[3])}; return __builtin_bit_cast(bf16x8, w); }
DI float rowmax4(float m) {
  { auto r = __builtin_amdgcn_permlane16_swap(__float_as_uint(m), __float_as_uint(m), false, false); m = MX3(__uint_as_float(r[0]), __uint_as_float(r[1]), m); }
  { auto r = __builtin_amdgcn_permlane32_swap(__float_as_uint(m), __float_as_uint(m), false, false); m = MX3(__uint_as_float(r[0]), __uint_as_float(r[1]), m); }
  return m;
}

struct St {
  bf16x8 qf[2][4];
  f32x4 o[2][8], ls[2];
  f32x4 ox[8]; float lx, mx;
  f32x4 s[2][2][2];
  f32x4 sx[2];
  f32x4 nm[2];
  u32x4 pw[2], pwx;
  bf16x8 kf[8], vf[8], qx[8], ones;
  float ma;
  lds_cptr bK, bV0, bV1, bQx;
};
DI void rdK(St& c, int g) { c.kf[g] = ldsb128(c.bK + 4096 * (g >> 2) + 512 * (g & 3)); }
DI void rdV(St& c, int d) { c.vf[d] = ldstr2(((d & 1) ? c.bV1 : c.bV0) + 512 * (d >> 1), 4096); }

template <int X, bool FIRST> DI void decide_main(St& c) {
  if (FIRST || __builtin_expect(__any(c.ma > THR), 0)) {
    SBAR(); NOP8(); SBAR();
#pragma unroll
    for (int a = 0; a < 2; ++a) {
      const f32x4 &u = c.s[X][0][a], &v = c.s[X][1][a];
      float m = rowmax4(MX3(MX3(MX3(u[0], u[1], u[2]), u[3], v[0]), MX3(v[1], v[2], v[3]), v[3]));
      if (!FIRST) m = fmaxf(m, 0.f);
      c.s[X][0][a] -= m; c.s[X][1][a] -= m; c.nm[a] -= m;
      if (!FIRST) {
        const float al = ex2(-m);
        c.ls[a] *= al;
#pragma unroll
        for (int d = 0; d < 8; ++d) c.o[a][d] *= al;
      }
    }
    SBAR(); NOP2(); SBAR();
  }
}
DI void decide_x(St& c) {
  const f32x4 &u = c.sx[0], &v = c.sx[1];
  const float lm = MX3(MX3(MX3(u[0], u[1], u[2]), u[3], v[0]), MX3(v[1], v[2], v[3]), v[3]);
  if (__builtin_expect(__any(lm - c.mx > THR), 0)) {
    SBAR(); NOP8(); SBAR();
    const float mn = fmaxf(rowmax4(lm), c.mx), al = ex2(c.mx - mn);
    c.mx = mn; c.lx *= al;
#pragma unroll
    for (int d = 0; d < 8; ++d) c.ox[d] *= al;
    SBAR(); NOP2(); SBAR();
  }
}

struct NoDma { DI void operator()(int) const {} };
template <int X, bool QK, bool XQ, bool EXP, bool XE, bool NV, class F = NoDma> DI void phaseA(St& c, F dma = F{}) {
  if (XQ) { c.qx[0] = ldsb128(c.bQx); c.qx[1] = ldsb128(c.bQx + 1024); }
#pragma unroll
  for (int g = 0; g < 8; ++g) {
    const int tau = g >> 2, s = g & 3;
    if (QK && g < 4) rdK(c, g + 4);
    if (XQ && g < 6) c.qx[g + 2] = ldsb128(c.bQx + 1024 * ((g + 2) & 3));
    if (NV && g >= 4) rdV(c, g - 4);
    dma(g);
    SBAR();
    if (QK) { if (s == 0) mmc(c.s[X ^ 1][tau][0], c.kf[g], c.qf[0][s], c.nm[0]); else mma(c.s[X ^ 1][tau][0], c.kf[g], c.qf[0][s]); }
    if (EXP) { f32x4& v = c.s[X][(g >> 1) & 1][g >> 2]; v[2 * (g & 1)] = ex2(v[2 * (g & 1)]); PIN(v); }
    SBAR();
    if (QK) { if (s == 0) mmc(c.s[X ^ 1][tau][1], c.kf[g], c.qf[1][s], c.nm[1]); else mma(c.s[X ^ 1][tau][1], c.kf[g], c.qf[1][s]); }
    if (EXP) { f32x4& v = c.s[X][(g >> 1) & 1][g >> 2]; v[2 * (g & 1) + 1] = ex2(v[2 * (g & 1) + 1]); PIN(v);
      if (g > 0) { const int p = g - 1; const f32x4& u = c.s[X][(p >> 1) & 1][p >> 2]; c.pw[p >> 2][p & 3] = cvtpk(u[2 * (p & 1)], u[2 * (p & 1) + 1]); PIN(c.pw[p >> 2]); } }
    SBAR();
    if (XQ) { if (s == 0) mm0(c.sx[tau], c.kf[g], c.qx[g]); else mma(c.sx[tau], c.kf[g], c.qx[g]); SBAR(); }
    if (XE) { f32x4& v = c.sx[g >> 2]; v[g & 3] = ex2(v[g & 3] - c.mx); c.lx += v[g & 3]; PIN(v); PIN(c.lx);
      if (g & 1) { c.pwx[g >> 1] = cvtpk(v[(g & 3) - 1], v[g & 3]); PIN(c.pwx); } SBAR(); }
  }
  if (EXP) { const f32x4& u = c.s[X][1][1]; c.pw[1][3] = cvtpk(u[2], u[3]); }
}
template <int X, bool MAX, bool XP, bool NK, class F = NoDma> DI void phaseB(St& c, F dma = F{}) {
  PIN(c.pw[1]); SBAR(); NOP2();
  const bf16x8 p0 = __builtin_bit_cast(bf16x8, c.pw[0]), p1 = __builtin_bit_cast(bf16x8, c.pw[1]);
#pragma unroll
  for (int g = 0; g < 8; ++g) {
    if (g < 4) rdV(c, g + 4);
    if (NK && g >= 4) rdK(c, g - 4);
    dma(g);
    SBAR();
    mma(c.o[0][g], c.vf[g], p0);
    if (MAX && g >= 2 && g < 6) {
      const f32x4& n = c.s[X ^ 1][(g - 2) >> 1][(g - 2) & 1];
      if (g == 2) c.ma = MX3(n[0], n[1], n[2]); else c.ma = MX3(c.ma, n[0], n[1]);
      PIN(c.ma);
    }
    SBAR();
    mma(c.o[1][g], c.vf[g], p1);
    if (MAX && g >= 2 && g < 6) {
      const f32x4& n = c.s[X ^ 1][(g - 2) >> 1][(g - 2) & 1];
      if (g == 2) c.ma = MX3(c.ma, n[3], n[3]); else c.ma = MX3(c.ma, n[2], n[3]);
      PIN(c.ma);
    }
    SBAR();
    if (XP) { mma(c.ox[g], c.vf[g], __builtin_bit_cast(bf16x8, c.pwx)); SBAR(); }
    if (g == 0) { mma(c.ls[0], c.ones, p0); SBAR(); }
    if (g == 1) { mma(c.ls[1], c.ones, p1); SBAR(); }
  }
}

typedef __attribute__((address_space(1))) unsigned gu32;
#define RLX_AGENT __ATOMIC_RELAXED, __HIP_MEMORY_SCOPE_AGENT
DI void ldo(bf16x8& d, unsigned voff, rsrc_t srd, unsigned soff) {
  asm volatile("buffer_load_dwordx4 %0, %1, %2, %3 offen sc1" : "=v"(d) : "v"(voff), "s"(srd), "s"(soff) : "memory");
}
DI void sto(bf16x8 v, unsigned voff, rsrc_t srd, unsigned off) { __builtin_amdgcn_raw_buffer_store_b128(__builtin_bit_cast(u32x4, v), srd, voff + off, 0, 16); }
#define XORV(r, k) asm volatile("v_xor_b32 %0, %1, %0" : "+v"(r) : "i"(k))
#define LDSW(T, p) (*(__attribute__((address_space(3))) T*)(p))
#define LDSR(T, p) (*(const __attribute__((address_space(3))) T*)(p))

DI unsigned lane_id_();
DI unsigned lane_id() { unsigned l; asm volatile("v_mbcnt_lo_u32_b32 %0, -1, 0\n\tv_mbcnt_hi_u32_b32 %0, -1, %0" : "=v"(l)); return l; }
DI unsigned opq(unsigned x) { asm volatile("" : "+s"(x)); return x; }
DI unsigned sel(unsigned a, unsigned b, unsigned long long m) { unsigned r; asm("v_cndmask_b32 %0, %1, %2, %3" : "=v"(r) : "v"(a), "v"(b), "s"(m)); return r; }
DI lds_cptr xor32(lds_cptr p) { return (lds_cptr)(uintptr_t)((unsigned)(uintptr_t)p ^ 32u); }
struct Id { unsigned w, g, hf, par, wq2, grp, jq, br, b, h0; };
DI Id mkid(unsigned wid, unsigned bid) {
  wid = opq(wid); bid = opq(bid);
  Id i; i.w = wid & 3; i.g = wid >> 2; i.hf = wid & 1; i.par = (wid >> 1) & 1; i.wq2 = wid >> 1;
  i.grp = 4 * (bid & 7) + ((bid >> 3) & 3); i.jq = bid >> 5; i.br = i.grp >> 4; i.b = i.grp & 15; i.h0 = 4 * i.jq; return i;
}
struct StageLd {
  rsrc_t srdX, srdW; unsigned voW, voX, m0, jq;
  DI void init(const Ptrs& P, unsigned wid, unsigned bid, unsigned lds0, unsigned rnd) {
    const Id i = mkid(wid, bid); const unsigned lane = lane_id(), lr = (lane >> 2) & 7, hi = lane >> 5, p4 = lane & 3;
    srdX = mkrsrc((const char*)P.x[i.br] + (size_t)i.b * SQ * DM * 2, SQ * DM * 2);
    srdW = mkrsrc((const char*)P.wq[i.br] + i.h0 * 256 + rnd * 512, DM * NH * DH * 2 - i.h0 * 256 - rnd * 512);
    voW = 8192u * (8 * i.hf + lr) + 16 * (8 * i.wq2 + 4 * hi + (p4 ^ (2 * i.hf)));
    voX = 1024u * (8 * i.hf + 32 * i.wq2 + lr) + 16 * (4 * hi + (p4 ^ (2 * i.hf)));
    m0 = lds0 + 1024 * i.wq2; jq = i.jq | (i.hf << 8);
  }
  DI void piece(unsigned step, int k) const {
    const unsigned hf = jq >> 8, ks = step, sb = STG * ((step + 2) % 3);
    if (k < 4) glds<false>(m0 + sb + 4096 * hf + 8192 * k, voW, srdW, ks * (64 * 8192) + k * (16 * 8192));
    else glds<false>(4 * m0 + sb + 32768 + 1024 * hf + 2048 * (k - 4), voX, srdX, ks * 128 + (k - 4) * (16 * 1024));
  }
  DI void operator()(unsigned step) const { _Pragma("unroll") for (int k = 0; k < 6; ++k) piece(step, k); }
};
DI unsigned uhead(const Id& i, unsigned n) { return 4 * n + 2 * (i.jq >> 2) + i.g; }
DI unsigned woSlot(unsigned g, unsigned hs) { return 32768u * ((0x2013u >> (4 * (hs & 3))) & 3u) + 16384 * g; }
struct WoLd {
  rsrc_t srd; unsigned vo, m0; Id i;
  DI void init(const Ptrs& P, unsigned wid, unsigned bid, unsigned lds0) {
    i = mkid(wid, bid); const unsigned lane = lane_id(), lr = (lane >> 2) & 7, hi = lane >> 5, p4 = lane & 3, blk = (lane >> 4) & 1;
    srd = mkrsrc((const char*)P.wo[i.br] + (i.jq & 3) * 256, 4096 * 1024 - (i.jq & 3) * 256);
    vo = 1024u * (8 * i.par + lr) + 16 * (4 * (2 * i.hf + hi) + (p4 ^ (2 * blk)));
    m0 = lds0 + 2048 * i.par + 1024 * i.hf;
  }
  DI void piece(unsigned hs, int k) const {
    const unsigned so = hs < 16 ? (uhead(i, hs >> 1) * 128 + 64 * (hs & 1)) * 1024 : 0x40000000u;
    glds<false>(m0 + woSlot(i.g, hs) + 4096 * k, vo, srd, so + k * (16 * 1024));
  }
  DI void operator()(unsigned hs) const { _Pragma("unroll") for (int k = 0; k < 4; ++k) piece(hs, k); }
};
#define LANEVARS const int lane = lane_id(), hi = lane >> 5, r16 = lane & 15, g4 = lane >> 4, qp = (lane & 15) >> 2, p4 = lane & 3, blk = (lane >> 4) & 1, lr = (lane >> 2) & 7, \
                 rowoff = 64 * (r16 & 7) + 16 * (g4 ^ (2 * (r16 >> 3))); (void)hi; (void)qp; (void)p4; (void)blk; (void)lr; (void)rowoff
#define IDVARS const Id id_ = mkid(wid, bid); const unsigned w = id_.w, g = id_.g, hf = id_.hf, par = id_.par, wq2 = id_.wq2, grp = id_.grp, jq = id_.jq, br = id_.br, b = id_.b, h0 = id_.h0; \
               (void)w; (void)g; (void)hf; (void)par; (void)wq2; (void)grp; (void)jq; (void)br; (void)b; (void)h0
#define AINL __attribute__((always_inline))
template <int N> using IC = std::integral_constant<int, N>;
DI void wait_count(gu32* cnt, unsigned want) {
  unsigned spins = 0;
  while ((unsigned)rfl(__hip_atomic_load(cnt, RLX_AGENT)) < want && ++spins < (1u << 16)) __builtin_amdgcn_s_sleep(1);
}

__global__ __launch_bounds__(512, 2) void fused_kernel(Ptrs P) {
  extern __shared__ __attribute__((aligned(16))) char smem[];
  const unsigned wid = rfl(threadIdx.x >> 6), bid = blockIdx.x;
  STAMP(0);
  const unsigned lds0 = (unsigned)(uintptr_t)smem;
  const lds_cptr L = (lds_cptr)smem;
  asm volatile("s_nop 4" ::: "memory");
  unsigned tdum = 0;
  { StageLd st; st.init(P, wid, bid, lds0, 0);
    st(0);
    IDVARS; LANEVARS;
    const unsigned voE = 1024u * (128 + 8 * hf + lr) + 16 * (4 * hi + (p4 ^ (2 * hf))) + 256 * wq2;
    _Pragma("unroll") for (int i = 0; i < 2; ++i) glds<false>(lds0 + XEO + 8192 * hf + 2048 * wq2 + 1024 * i, voE, st.srdX, 128u * i);
    st(1); st(2);
    const unsigned wb = bid >> 7, wi = bid & 127;
    if (wid < 4) { const rsrc_t sw = mkrsrc(P.wo[wb], 4096 * 1024);
      asm volatile("buffer_load_dword %0, %1, %2, %3 offen" : "+v"(tdum) : "v"(128u * lane), "s"(sw), "s"(32768u * wi + 8192u * wid) : "memory"); }
    else { const rsrc_t sw = mkrsrc(P.wq[wb], 4096 * 1024);
      asm volatile("buffer_load_dword %0, %1, %2, %3 offen" : "+v"(tdum) : "v"(1024u * ((lane >> 2) & 7) + 512u + 128u * (lane & 3)), "s"(sw), "s"(8192u * (4 * wi + wid - 4)) : "memory"); }
  }

#pragma unroll 1
  for (unsigned rnd = 0; rnd < 2; ++rnd) {
    St c;
    IDVARS;
    const rsrc_t srdK = mkrsrc((const char*)P.k[br] + (size_t)(b * NH + h0 + 2 * rnd + g) * SK * DH * 2, SK * DH * 2);
    const rsrc_t srdV = mkrsrc((const char*)P.v[br] + (size_t)(b * NH + h0 + 2 * rnd + g) * SK * DH * 2, SK * DH * 2);
    unsigned voK, voV;
    { LANEVARS; voK = 256u * (8 * par + lr) + 16 * (4 * (2 * hf + hi) + (p4 ^ (2 * par))); voV = 256u * (8 * par + lr) + 16 * (4 * (2 * hf + hi) + (p4 ^ (2 * blk))); }
    const unsigned m0T = lds0 + 16384 * g + 2048 * par + 1024 * hf;
    auto pcK = [&](unsigned t, int i) AINL { glds<true>(m0T + 65536 * ((t + 1) & 1) + 4096 * i, voK, srdK, t * 16384 + i * 4096); };
    auto pcV = [&](unsigned t, int i) AINL { glds<true>(m0T + 32768 + 65536 * ((t + 1) & 1) + 4096 * i, voV, srdV, t * 16384 + i * 4096); };
    {
      LANEVARS;
      StageLd st; st.init(P, wid, bid, lds0, rnd);
      lds_cptr bW[4];
#pragma unroll
      for (int j = 0; j < 4; ++j) bW[j] = L + 4096 * g4 + 2048 * g + 512 * ((j + w) & 3) + 64 * qp + 32 * (g4 & 1) + 8 * p4;
      const lds_cptr bX = L + 32768 + 1024 * (4 * w + (r16 >> 3)) + rowoff;
      const lds_cptr bE = L + XEO + 8192 * (r16 >> 3) + rowoff;
      f32x4 qa[2][8] = {}, qx[2] = {};
#pragma unroll
      for (int d = 0; d < 8; ++d) { PIN(qa[0][d]); PIN(qa[1][d]); }
      PIN(qx[0]); PIN(qx[1]);
      constexpr int PD = 6;
      bf16x8 a[16], xb[2][2], xe[2];
      lds_cptr pW0[4], pW1[4], pX, pE;
      auto setp = [&](unsigned step) AINL {
        const unsigned sb = STG * ((step + 2) % 3);
#pragma unroll
        for (int j = 0; j < 4; ++j) { pW0[j] = bW[j] + sb; pW1[j] = xor32(pW0[j]); }
        pX = bX + sb; pE = bE + 1024 * step;
      };
      auto rdA = [&](int m) AINL {
        if ((m & 7) == 0) { xb[m >> 3][0] = ldsb128(pX + 512 * (m >> 3)); xb[m >> 3][1] = ldsb128(pX + 2048 + 512 * (m >> 3)); xe[m >> 3] = ldsb128(pE + 512 * (m >> 3)); }
        a[m] = ldstr2(((m & 1) ? pW1[(m & 7) >> 1] : pW0[(m & 7) >> 1]) + 16384 * (m >> 3), 256);
      };
      unsigned tl = 0; (void)tl;
      auto qstep = [&](unsigned step, auto KINDC) AINL {
        constexpr int KIND = decltype(KINDC)::value;
        TLOG(tl, step);
#pragma unroll
        for (int m = 0; m < 16; ++m) {
          if (m == 16 - PD) {
            if (KIND == 6) { WAIT_BAR(4); } else if (KIND == 7) { WAIT_BAR(8); } else { WAIT_BAR(6); }
            if (KIND != 7) setp(step + 1);
            if (KIND == 5 && rnd == 1 && wid == 0 && lane == 0) __hip_atomic_fetch_add((gu32*)((char*)P.ws + WS_CNT) + grp * 256, 1u, RLX_AGENT);
          }
          if (m + PD < 16) rdA(m + PD); else if (KIND != 7) rdA(m + PD - 16);
          if (m >= 16 - PD) { if (KIND == 0) st.piece(step + 3, m - (16 - PD)); else if (m < 20 - PD) { if (KIND == 5) pcK(0, m - (16 - PD)); else if (KIND == 6) pcV(0, m - (16 - PD)); else pcK(1, m - (16 - PD)); } }
          SBAR();
          mma(qa[0][m & 7], a[m], xb[m >> 3][0]);
          mma(qa[1][m & 7], a[m], xb[m >> 3][1]);
          if ((m & 7) < 2) mma(qx[m & 1], a[m], xe[m >> 3]);
          SBAR();
        }
      };
      setp(0);
      if (rnd == 1) { WAIT_BAR(21); } else { WAIT_BAR(13); }
#pragma unroll
      for (int m = 0; m < PD; ++m) rdA(m);
      SBAR();
#pragma unroll 1
      for (unsigned step = 0; step < 5; ++step) qstep(step, IC<0>{});
      PIN(tdum);
      qstep(5, IC<5>{}); qstep(6, IC<6>{}); qstep(7, IC<7>{});
      TLOG(tl, 8);
      SBAR(); NOP8(); SBAR();
      unsigned qr[2][4][4];
#pragma unroll
      for (int a = 0; a < 2; ++a)
#pragma unroll
        for (int s = 0; s < 4; ++s) {
          const f32x4 u = qa[a][2 * s] * C2, v = qa[a][2 * s + 1] * C2;
          unsigned A0 = cvtpk(u[0], u[1]), A1 = cvtpk(u[2], u[3]), B0 = cvtpk(v[0], v[1]), B1 = cvtpk(v[2], v[3]);
          auto r0 = __builtin_amdgcn_permlane32_swap(A0, B0, false, false); auto r1 = __builtin_amdgcn_permlane32_swap(A1, B1, false, false);
          auto t0 = __builtin_amdgcn_permlane16_swap(r0[0], r0[1], false, false); auto t1 = __builtin_amdgcn_permlane16_swap(r1[0], r1[1], false, false);
          qr[a][s][0] = t0[0]; qr[a][s][1] = t1[0]; qr[a][s][2] = t0[1]; qr[a][s][3] = t1[1];
        }
      const unsigned long long m1 = (w & 1) ? ~0ull : 0ull, m2 = (w & 2) ? ~0ull : 0ull;
#pragma unroll
      for (int a = 0; a < 2; ++a) {
        unsigned t[4][4]; u32x4 q[4];
#pragma unroll
        for (int s = 0; s < 4; ++s)
#pragma unroll
          for (int e = 0; e < 4; ++e) t[s][e] = sel(qr[a][s][e], qr[a][(s + 3) & 3][e], m1);
#pragma unroll
        for (int s = 0; s < 4; ++s) {
#pragma unroll
          for (int e = 0; e < 4; ++e) q[s][e] = sel(t[s][e], t[(s + 2) & 3][e], m2);
          c.qf[a][s] = __builtin_bit_cast(bf16x8, q[s]);
        }
      }
#pragma unroll
      for (int u = 0; u < 2; ++u) {
        const f32x4 v = qx[u] * C2;
        u32x2 q = {cvtpk(v[0], v[1]), cvtpk(v[2], v[3])};
        LDSW(u32x2, (lds_ptr)smem + QXO + 4096 * g + 1024 * w + (16 * (2 * u + (g4 >> 1)) + r16) * 16 + 8 * (g4 & 1)) = q;
      }
    }
    STAMP(1 + 4 * rnd);

#define TOGK(k) XORV(c.bK, k)
#define TOGV(k) do { XORV(c.bV0, k); XORV(c.bV1, k); } while (0)
#define TILE(XC, XN, GLDS_STMT, LASTT) do { \
      WAIT_BAR(0); \
      _Pragma("unroll") for (int m = 0; m < 4; ++m) rdV(c, m); \
      SBAR(); \
      TOGK(0x12000); \
      phaseB<0, true, XC, !LASTT>(c, [&](int q) AINL { GLDS_STMT; }); decide_main<1, false>(c); \
      TOGV(0x2000); \
      phaseA<1, !LASTT, XN && !LASTT, true, false, true>(c); \
      TOGK(0x2000); \
      phaseB<1, !LASTT, false, !LASTT>(c); \
      TOGV(0x12000); \
      if (!LASTT) { decide_main<0, false>(c); if (XN) decide_x(c); phaseA<0, true, false, true, XN, false>(c); } \
    } while (0)
    auto attention = [&](auto PARC) AINL {
      constexpr bool XE = decltype(PARC)::value == 0, XO = !XE;
      { LANEVARS;
        c.bK  = L + 65536 + 16384 * g + 8192 * hf + 2048 * (r16 >> 3) + rowoff;
        c.bV0 = L + 65536 + 32768 + 16384 * g + 8192 * hf + 2048 * (g4 >> 1) + 256 * (g4 & 1) + 64 * qp + 32 * (g4 & 1) + 8 * p4;
        c.bV1 = xor32(c.bV0);
        c.bQx = L + QXO + 4096 * g + 16 * lane; }
#pragma unroll
      for (int d = 0; d < 8; ++d) { c.o[0][d] = f32x4{}; c.o[1][d] = f32x4{}; c.ox[d] = f32x4{}; }
      c.ls[0] = c.ls[1] = c.nm[0] = c.nm[1] = f32x4{};
#pragma unroll
      for (int d = 0; d < 8; ++d) { PIN(c.o[0][d]); PIN(c.o[1][d]); PIN(c.ox[d]); }
      PIN(c.ls[0]); PIN(c.ls[1]); PIN(c.nm[0]); PIN(c.nm[1]);
      c.mx = -INFINITY; c.lx = 0.f;
      { u32x4 one = {0x3F803F80u, 0x3F803F80u, 0x3F803F80u, 0x3F803F80u}; asm volatile("" : "+v"(one)); c.ones = __builtin_bit_cast(bf16x8, one); }
      WAIT_BAR(8);
#pragma unroll
      for (int s = 0; s < 4; ++s) rdK(c, s);
      SBAR();
      phaseA<1, true, XE, false, false, false>(c);
      TOGK(0x2000);
#pragma unroll
      for (int s = 0; s < 4; ++s) rdK(c, s);
      decide_main<0, true>(c); if (XE) decide_x(c);
      phaseA<0, true, false, true, XE, false>(c);
      STAMP(2 + 4 * rnd);
#pragma unroll 1
      for (unsigned t = 0; t < 6; t += 2) {
        TILE(XE, XO, { if (q < 4) pcV(t + 1, q); else pcK(t + 2, q - 4); }, false);
        TILE(XO, XE, { if (q < 4) pcV(t + 2, q); else pcK(t + 3, q - 4); }, false);
      }
      TILE(XE, XO, { if (q < 4) pcV(7, q); }, false);
      TILE(XO, XE, { if (q == 1) { if (rnd == 0) { StageLd st; st.init(P, wid, bid, lds0, 1); st(0); } else { WoLd wo; wo.init(P, wid, bid, lds0); wo(0); } } }, true);
      SBAR(); NOP8(); SBAR();
      STAMP(3 + 4 * rnd);
      LANEVARS;
      const lds_ptr scr = (lds_ptr)smem + 65536 * g;
      c.lx = swapadd32(swapadd16(c.lx));
#pragma unroll
      for (int d = 0; d < 8; ++d) LDSW(f32x4, scr + 8192 * w + 1024 * d + 16 * lane) = c.ox[d];
      const f32x4 own0 = LDSR(f32x4, scr + 10240 * w + 16 * lane), own1 = LDSR(f32x4, scr + 10240 * w + 1024 + 16 * lane);
      asm volatile("" ::: "memory");
      { f32x2 ml = {c.mx, c.lx}; LDSW(f32x2, scr + 10240 * w + 8 * lane) = ml; }
      const rsrc_t srdO = mkrsrc((const char*)P.ws + WS_O + (size_t)(opq(grp) * NH + h0 + 2 * rnd + g) * OITEM, OITEM);
#pragma unroll
      for (int a = 0; a < 2; ++a) {
        const float inv = __builtin_amdgcn_rcpf(c.ls[a][0]);
#pragma unroll
        for (int u = 0; u < 4; ++u) sto(pack44(c.o[a][2 * u] * inv, c.o[a][2 * u + 1] * inv), 8192 * w + 16 * lane, srdO, 4096 * a + 1024 * u);
      }
      asm volatile("s_waitcnt lgkmcnt(0)\n\ts_barrier" ::: "memory");
      {
        f32x2 ml[4];
#pragma unroll
        for (int i = 0; i < 4; ++i) ml[i] = LDSR(f32x2, scr + 10240 * i + 8 * r16);
        const float m = fmaxf(fmaxf(ml[0][0], ml[1][0]), fmaxf(ml[2][0], ml[3][0]));
        float f[4], lt = 0.f;
#pragma unroll
        for (int i = 0; i < 4; ++i) { f[i] = ex2(ml[i][0] - m); lt += f[i] * ml[i][1]; }
        const float inv = __builtin_amdgcn_rcpf(lt);
        f32x4 a0 = {}, a1 = {};
#pragma unroll
        for (int i = 0; i < 4; ++i) {
          f32x4 t0 = LDSR(f32x4, scr + 8192 * i + 2048 * w + 16 * lane), t1 = LDSR(f32x4, scr + 8192 * i + 2048 * w + 1024 + 16 * lane);
          if (w == (unsigned)i) { t0 = own0; t1 = own1; }
          a0 += t0 * f[i]; a1 += t1 * f[i];
        }
        sto(pack44(a0 * inv, a1 * inv), 32768 + 1024 * w + 16 * lane, srdO, 0);
      }
      asm volatile("s_waitcnt lgkmcnt(0)\n\ts_barrier" ::: "memory");
      if (rnd == 0) { StageLd st; st.init(P, wid, bid, lds0, 1); st(1); st(2); } else { WoLd wo; wo.init(P, wid, bid, lds0); wo(1); wo(2); }
    };
    if (par == 0) attention(IC<0>{}); else attention(IC<1>{});
    STAMP(4 + 4 * rnd);
  }

  IDVARS; LANEVARS;
  gu32* cnt = (gu32*)((char*)P.ws + WS_CNT) + grp * 256;
  const unsigned typ = jq >> 2, ns = jq & 3;
  {
    asm volatile("s_waitcnt vmcnt(8) lgkmcnt(0)\n\ts_barrier" ::: "memory");
    if (wid == 0) {
      if (lane == 0) __hip_atomic_fetch_add(cnt + 32, 1u, RLX_AGENT);
      wait_count(cnt + 32 * typ, 8u);
    }
    asm volatile("s_barrier" ::: "memory");
  }
  STAMP(9);

  {
    f32x4 acc[2][8] = {}, accx[8] = {};
    auto unit = [&](auto PARC) AINL {
      constexpr int XV = decltype(PARC)::value;
      WoLd wo; wo.init(P, wid, bid, lds0);
      const rsrc_t srdO = mkrsrc((const char*)P.ws + WS_O + (size_t)grp * NH * OITEM, NH * OITEM);
      const unsigned voO0 = 8192 * w + 1024 * hf + 16 * lane, voO1 = voO0 ^ 1024u, voOx = 32768 + 1024 * w + 16 * lane;
      bf16x8 of[4][5];
      auto pcO = [&](unsigned hs, auto SETC, int k) AINL {
        constexpr int set = decltype(SETC)::value;
        const unsigned so = hs < 16 ? uhead(id_, hs >> 1) * OITEM : 0x40000000u;
        if (k < 4) ldo(of[set][k], (k & 2) ? voO1 : voO0, srdO, so + 2048 * (set & 1) + 4096 * (k & 1));
        else if ((set & 1) == XV) ldo(of[set][4], voOx, srdO, so);
      };
      auto ldO = [&](unsigned hs, auto SETC) AINL { _Pragma("unroll") for (int k = 0; k < 5; ++k) pcO(hs, SETC, k); };
      const lds_cptr bA0 = L + 8192 * hf + 2048 * (g4 >> 1) + 256 * (g4 & 1) + 64 * qp + 32 * (g4 & 1) + 8 * p4;
#pragma unroll
      for (int d = 0; d < 8; ++d) { PIN(acc[0][d]); PIN(acc[1][d]); PIN(accx[d]); }
      ldO(0, IC<0>{}); ldO(1, IC<1>{}); ldO(2, IC<2>{});
      constexpr int PD = 6;
      bf16x8 a[16];
      lds_cptr pA0, pA1, pB0, pB1;
      auto setp = [&](int set) AINL { pA0 = bA0 + woSlot(g, set); pA1 = xor32(pA0); pB0 = (lds_cptr)(uintptr_t)((unsigned)(uintptr_t)pA0 ^ 8192u); pB1 = xor32(pB0); };
      auto rdA = [&](int m) AINL { a[m] = ldstr2(((m >> 3) ? ((m & 1) ? pB1 : pB0) : ((m & 1) ? pA1 : pA0)) + 512 * ((m & 7) >> 1), 4096); };
#define OWAIT(N, S) do { if (((S) & 1) == XV) asm volatile("s_waitcnt vmcnt(" #N ")\n\ts_barrier" : "+v"(of[S][0]), "+v"(of[S][1]), "+v"(of[S][2]), "+v"(of[S][3]), "+v"(of[S][4]) :: "memory"); \
                         else asm volatile("s_waitcnt vmcnt(" #N ")\n\ts_barrier" : "+v"(of[S][0]), "+v"(of[S][1]), "+v"(of[S][2]), "+v"(of[S][3]) :: "memory"); } while (0)
      unsigned tl = 0; (void)tl;
      auto ustep = [&](unsigned hs, auto SETC) AINL {
        constexpr int set = decltype(SETC)::value, nset = (set + 1) & 3;
        TLOG(tl, hs);
#pragma unroll
        for (int m = 0; m < 16; ++m) {
          if (m == 16 - PD) { if (set == 0) OWAIT(9, nset); else OWAIT(13, nset); setp(nset); }
          rdA((m + PD) & 15);
          if (m < 5) pcO(hs + 3, IC<(set + 3) & 3>{}, m); else if (m >= 16 - PD && m < 20 - PD) wo.piece(hs + 3, m - (16 - PD));
          SBAR();
          mma(acc[0][m & 7], a[m], of[set][2 * (m >> 3)]);
          mma(acc[1][m & 7], a[m], of[set][2 * (m >> 3) + 1]);
          if ((set & 1) == XV && m < 8) mma(accx[m], a[m], of[set][4]);
          SBAR();
        }
      };
      setp(0);
#pragma unroll
      for (int m = 0; m < PD; ++m) rdA(m);
      OWAIT(9, 0);
#pragma unroll 1
      for (unsigned hs = 0; hs < 16; hs += 4) { ustep(hs, IC<0>{}); ustep(hs + 1, IC<1>{}); ustep(hs + 2, IC<2>{}); ustep(hs + 3, IC<3>{}); }
      TLOG(tl, 16);
      asm volatile("s_waitcnt vmcnt(0)" : "+v"(of[0][0]), "+v"(of[0][1]), "+v"(of[0][2]), "+v"(of[0][3]), "+v"(of[1][0]), "+v"(of[1][1]), "+v"(of[1][2]), "+v"(of[1][3]),
                                           "+v"(of[2][0]), "+v"(of[2][1]), "+v"(of[2][2]), "+v"(of[2][3]), "+v"(of[XV][4]), "+v"(of[XV == 0 ? 2 : 1][4]) :: "memory");
      SBAR(); NOP8(); SBAR();
    };
    if (par == 0) unit(IC<0>{}); else unit(IC<1>{});
    STAMP(10);
    asm volatile("s_waitcnt lgkmcnt(0)\n\ts_barrier" ::: "memory");
    const lds_ptr red = (lds_ptr)smem + 8192 * wid + 16 * lane, redx = (lds_ptr)smem + 65536 + 16 * lane;
    f32x4 mine[8];
    if (g == 0) {
#pragma unroll
      for (int i = 0; i < 8; ++i) { LDSW(f32x4, red + 1024 * i) = acc[1][i]; mine[i] = acc[0][i]; PIN(mine[i]); }
    } else {
#pragma unroll
      for (int i = 0; i < 8; ++i) { LDSW(f32x4, red + 1024 * i) = acc[0][i]; mine[i] = acc[1][i]; PIN(mine[i]); }
    }
#pragma unroll
    for (int i = 0; i < 8; ++i) LDSW(f32x4, redx + 8192 * wid + 1024 * i) = accx[i];
    if (typ == 1 && wid == 0) wait_count(cnt + 64 + 32 * ns, 1u);
    asm volatile("s_waitcnt lgkmcnt(0)\n\ts_barrier" ::: "memory");
    const rsrc_t srdP = mkrsrc((const char*)P.ws + WS_PART + (size_t)(grp * 4 + ns) * PARTB, PARTB);
    u32x4 pp[9];
    if (typ == 1) {
#pragma unroll
      for (int i = 0; i < 9; ++i) pp[i] = __builtin_amdgcn_raw_buffer_load_b128(srdP, 9216 * wid + 16 * lane + 1024 * i, 0, 16);
    }
    f32x4 vx = {};
#pragma unroll
    for (int i = 0; i < 8; ++i) vx += LDSR(f32x4, redx + 8192 * i + 1024 * wid);
    { const lds_cptr oth = L + 8192 * (wid ^ 4) + 16 * lane;
#pragma unroll
      for (int i = 0; i < 8; ++i) mine[i] += LDSR(f32x4, oth + 1024 * i); }
    if (typ == 0) {
#pragma unroll
      for (int i = 0; i < 8; ++i) __builtin_amdgcn_raw_buffer_store_b128(__builtin_bit_cast(u32x4, mine[i]), srdP, 9216 * wid + 16 * lane + 1024 * i, 0, 16);
      __builtin_amdgcn_raw_buffer_store_b128(__builtin_bit_cast(u32x4, vx), srdP, 9216 * wid + 16 * lane + 8192, 0, 16);
      asm volatile("s_waitcnt vmcnt(0)\n\ts_barrier" ::: "memory");
      if (wid == 0 && lane == 0) __hip_atomic_store(cnt + 64 + 32 * ns, 1u, RLX_AGENT);
    } else {
      const lds_ptr tile = (lds_ptr)smem + 131072 - 39168;
      asm volatile("s_waitcnt lgkmcnt(0)\n\ts_barrier" ::: "memory");
#pragma unroll
      for (int i = 0; i < 8; ++i) {
        const f32x4 v = mine[i] + __builtin_bit_cast(f32x4, pp[i]);
        u32x2 px = {cvtpk(v[0], v[1]), cvtpk(v[2], v[3])};
        LDSW(u32x2, tile + (32 * w + 16 * g + r16) * 272 + (16 * i + 4 * g4) * 2) = px;
      }
      { const f32x4 v = vx + __builtin_bit_cast(f32x4, pp[8]); u32x2 px = {cvtpk(v[0], v[1]), cvtpk(v[2], v[3])};
        LDSW(u32x2, tile + (128 + r16) * 272 + (16 * wid + 4 * g4) * 2) = px; }
      asm volatile("s_waitcnt lgkmcnt(0)\n\ts_barrier" ::: "memory");
      char* outb = (char*)P.out + ((size_t)b * SQ * 1024 + br * 512 + 128 * ns) * 2;
#pragma unroll
      for (int i = 0; i < 5; ++i) {
        const int row = 32 * i + 4 * wid + g4;
        if (row < SQ) *(u32x4*)(outb + (size_t)row * 2048 + 16 * r16) = LDSR(u32x4, tile + row * 272 + 16 * r16);
      }
    }
  }
  STAMP(11);
}

extern "C" void kernel_launch(void* const* d_in, const int* in_sizes, int n_in, void* d_out, int out_size, void* d_ws, size_t ws_size, hipStream_t stream) {
  (void)in_sizes; (void)n_in; (void)out_size; (void)ws_size;
  Ptrs P;
  for (int br = 0; br < 2; ++br) { P.x[br] = d_in[5 * br]; P.k[br] = d_in[5 * br + 1]; P.v[br] = d_in[5 * br + 2]; P.wq[br] = d_in[5 * br + 3]; P.wo[br] = d_in[5 * br + 4]; }
  P.out = d_out; P.ws = d_ws;
  (void)hipFuncSetAttribute((const void*)fused_kernel, hipFuncAttributeMaxDynamicSharedMemorySize, LDSB);
  (void)hipMemsetAsync((char*)d_ws + WS_CNT, 0, 32768, stream);
  hipLaunchKernelGGL(fused_kernel, dim3(256), dim3(512), LDSB, stream, P);
}
```

```cpp
#include <hip/hip_runtime.h>
#include <hip/hip_bf16.h>
#include <cstdint>
#include <type_traits>
#define CODEWARM 54272

typedef __bf16   bf16x8 __attribute__((ext_vector_type(8)));
typedef __bf16   bf16x4 __attribute__((ext_vector_type(4)));
typedef __bf16   bf16x2 __attribute__((ext_vector_type(2)));
typedef short    s16x4  __attribute__((ext_vector_type(4)));
typedef float    f32x4  __attribute__((ext_vector_type(4)));
typedef float    f32x2  __attribute__((ext_vector_type(2)));
typedef unsigned u32x4  __attribute__((ext_vector_type(4)));
typedef unsigned u32x2  __attribute__((ext_vector_type(2)));
typedef __attribute__((address_space(3))) char* lds_ptr;
typedef __attribute__((address_space(3))) const char* lds_cptr;
typedef __amdgpu_buffer_rsrc_t rsrc_t;

#define DI __device__ __forceinline__
#define WAIT_BAR(N) asm volatile("s_waitcnt vmcnt(" #N ") lgkmcnt(0)\n\ts_barrier" ::: "memory")
#define SBAR() __builtin_amdgcn_sched_barrier(0)

constexpr int NB = 16, SQ = 144, DM = 512, NH = 32, SK = 512, DH = 128;
constexpr float C2 = 0.08838834764831845f * 1.4426950408889634f;
constexpr float THR = 8.0f;
constexpr int LDSB = 163840, QXO = 131072, XEO = 147456;
constexpr int wslot(int s) { return s % 3 == 0 ? 32768 : s % 3 == 1 ? 65536 : 0; }
constexpr int xslot(int s) { return s % 3 == 0 ? 131072 : s % 3 == 1 ? 98304 : 114688; }
constexpr size_t WS_CNT = 0, WS_O = 131072, WS_PART = WS_O + (size_t)1024 * 36 * 1024;
constexpr int PARTB = 72 * 1024;
constexpr int OITEM = 36 * 1024;

DI unsigned rfl(unsigned x) { return __builtin_amdgcn_readfirstlane(x); }
DI unsigned cvtpk(float lo, float hi) { f32x2 v = {lo, hi}; bf16x2 b = __builtin_convertvector(v, bf16x2); return __builtin_bit_cast(unsigned, b); }
DI rsrc_t mkrsrc(const void* p, unsigned bytes) { return __builtin_amdgcn_make_buffer_rsrc((void*)p, (short)0, (int)bytes, 0x00020000); }
template <bool NT> DI void glds(unsigned mb, int mi, unsigned voff, rsrc_t srd, unsigned sb, int si) {
  unsigned t;
  if constexpr (NT) asm volatile("s_add_u32 m0, %1, %2\n\ts_add_u32 %0, %5, %6\n\tbuffer_load_dwordx4 %3, %4, %0 offen nt lds" : "=&s"(t) : "s"(mb), "i"(mi), "v"(voff), "s"(srd), "s"(sb), "i"(si) : "memory", "scc");
  else              asm volatile("s_add_u32 m0, %1, %2\n\ts_add_u32 %0, %5, %6\n\tbuffer_load_dwordx4 %3, %4, %0 offen lds" : "=&s"(t) : "s"(mb), "i"(mi), "v"(voff), "s"(srd), "s"(sb), "i"(si) : "memory", "scc");
}
DI bf16x8 ldsb128(lds_cptr p) { return *(const __attribute__((address_space(3))) bf16x8*)p; }
DI bf16x4 ldstr(lds_cptr p) { return __builtin_bit_cast(bf16x4, __builtin_amdgcn_ds_read_tr16_b64_v4i16((__attribute__((address_space(3))) s16x4*)p)); }
DI bf16x8 cat(bf16x4 a, bf16x4 b) { return __builtin_shufflevector(a, b, 0, 1, 2, 3, 4, 5, 6, 7); }
DI float swapadd32(float m) { auto r = __builtin_amdgcn_permlane32_swap(__float_as_uint(m), __float_as_uint(m), false, false); return __uint_as_float(r[0]) + __uint_as_float(r[1]); }
DI float swapadd16(float m) { auto r = __builtin_amdgcn_permlane16_swap(__float_as_uint(m), __float_as_uint(m), false, false); return __uint_as_float(r[0]) + __uint_as_float(r[1]); }

struct Ptrs { const void* x[2]; const void* k[2]; const void* v[2]; const void* wq[2]; const void* wo[2]; void* out; void* ws; };


#define PIN(x) asm volatile("" : "+v"(x))
#define MX3(a, b, c) __builtin_fmaxf(__builtin_fmaxf((a), (b)), (c))
DI void mma(f32x4& acc, bf16x8 a, bf16x8 b) { asm volatile("v_mfma_f32_16x16x32_bf16 %0, %1, %2, %0" : "+v"(acc) : "v"(a), "v"(b)); }
DI void mmc(f32x4& d, bf16x8 a, bf16x8 b, const f32x4& cin) { asm volatile("v_mfma_f32_16x16x32_bf16 %0, %1, %2, %3" : "=&v"(d) : "v"(a), "v"(b), "v"(cin)); }
DI void mm0(f32x4& d, bf16x8 a, bf16x8 b) { asm volatile("v_mfma_f32_16x16x32_bf16 %0, %1, %2, 0" : "=&v"(d) : "v"(a), "v"(b)); }
#define NOP2() asm volatile("s_nop 1")
#define NOP8() asm volatile("s_nop 7")
DI float ex2(float v) { return __builtin_amdgcn_exp2f(v); }
DI bf16x8 pack44(const f32x4& a, const f32x4& b) { u32x4 w = {cvtpk(a[0], a[1]), cvtpk(a[2], a[3]), cvtpk(b[0], b[1]), cvtpk(b[2], b[3])}; return __builtin_bit_cast(bf16x8, w); }
DI float rowmax4(float m) {
  { auto r = __builtin_amdgcn_permlane16_swap(__float_as_uint(m), __float_as_uint(m), false, false); m = MX3(__uint_as_float(r[0]), __uint_as_float(r[1]), m); }
  { auto r = __builtin_amdgcn_permlane32_swap(__float_as_uint(m), __float_as_uint(m), false, false); m = MX3(__uint_as_float(r[0]), __uint_as_float(r[1]), m); }
  return m;
}

DI lds_cptr xor1k(lds_cptr p) { return (lds_cptr)(uintptr_t)((unsigned)(uintptr_t)p ^ 1024u); }
struct St {
  bf16x8 qf[2][4];
  f32x4 o[2][8], ls[2];
  f32x4 ox[8]; float lx, mx;
  f32x4 s[2][2][2];
  f32x4 sx[2];
  f32x4 nm[2];
  u32x4 pw[2], pwx;
  bf16x8 kf[8], qx[8], ones; bf16x4 vf[8][2];
  float ma;
  lds_cptr bK0, bK1, bV0, bV1, bQx;
};
template <int PV> DI void rdK(St& c, int g) { c.kf[g] = ldsb128(((g & 1) ? c.bK1 : c.bK0) + 4096 * (g >> 2) + 1024 * (((g >> 1) & 1) ^ PV)); }
DI void rdV(St& c, int d, int t) { c.vf[d][t] = ldstr(((d & 1) ? c.bV1 : c.bV0) + 512 * (d >> 1) + 4096 * t); }

template <int X, bool FIRST> DI void decide_main(St& c) {
  if (FIRST || __builtin_expect(__any(c.ma > THR), 0)) {
    SBAR(); NOP8(); SBAR();
#pragma unroll
    for (int a = 0; a < 2; ++a) {
      const f32x4 &u = c.s[X][0][a], &v = c.s[X][1][a];
      float m = rowmax4(MX3(MX3(MX3(u[0], u[1], u[2]), u[3], v[0]), MX3(v[1], v[2], v[3]), v[3]));
      if (!FIRST) m = fmaxf(m, 0.f);
      c.s[X][0][a] -= m; c.s[X][1][a] -= m; c.nm[a] -= m;
      if (!FIRST) {
        const float al = ex2(-m);
        c.ls[a] *= al;
#pragma unroll
        for (int d = 0; d < 8; ++d) c.o[a][d] *= al;
      }
    }
    SBAR(); NOP2(); SBAR();
  }
}
DI void decide_x(St& c) {
  const f32x4 &u = c.sx[0], &v = c.sx[1];
  const float lm = MX3(MX3(MX3(u[0], u[1], u[2]), u[3], v[0]), MX3(v[1], v[2], v[3]), v[3]);
  if (__builtin_expect(__any(lm - c.mx > THR), 0)) {
    SBAR(); NOP8(); SBAR();
    const float mn = fmaxf(rowmax4(lm), c.mx), al = ex2(c.mx - mn);
    c.mx = mn; c.lx *= al;
#pragma unroll
    for (int d = 0; d < 8; ++d) c.ox[d] *= al;
    SBAR(); NOP2(); SBAR();
  }
}

struct NoDma { DI void operator()(int) const {} };
template <int PV, int X, bool QK, bool XQ, bool EXP, bool XE, int NV, class F = NoDma> DI void phaseA(St& c, F dma = F{}) {
  if (XQ) { c.qx[0] = ldsb128(c.bQx + 2048 * PV); c.qx[1] = ldsb128(xor1k(c.bQx) + 2048 * PV); }
#pragma unroll
  for (int g = 0; g < 8; ++g) {
    const int tau = g >> 2, s = g & 3;
    if (NV == 2 && g == 4) WAIT_BAR(0);
    SBAR();
    if (QK) { if (s == 0) mmc(c.s[X ^ 1][tau][0], c.kf[g], c.qf[0][s], c.nm[0]); else mma(c.s[X ^ 1][tau][0], c.kf[g], c.qf[0][s]); }
    SBAR();
    if (QK && g < 4) rdK<PV>(c, g + 4);
    if (NV && g >= 4) rdV(c, g - 4, 0);
    if (EXP) { f32x4& v = c.s[X][(g >> 1) & 1][g >> 2]; v[2 * (g & 1)] = ex2(v[2 * (g & 1)]); PIN(v); }
    SBAR();
    if (QK) { if (s == 0) mmc(c.s[X ^ 1][tau][1], c.kf[g], c.qf[1][s], c.nm[1]); else mma(c.s[X ^ 1][tau][1], c.kf[g], c.qf[1][s]); }
    SBAR();
    if (NV && g >= 4) rdV(c, g - 4, 1);
    if (XQ && g < 6) c.qx[g + 2] = ldsb128(((g & 1) ? xor1k(c.bQx) : c.bQx) + 2048 * ((((g + 2) >> 1) & 1) ^ PV));
    dma(g);
    if (EXP) { f32x4& v = c.s[X][(g >> 1) & 1][g >> 2]; v[2 * (g & 1) + 1] = ex2(v[2 * (g & 1) + 1]); PIN(v);
      if (g > 0) { const int p = g - 1; const f32x4& u = c.s[X][(p >> 1) & 1][p >> 2]; c.pw[p >> 2][p & 3] = cvtpk(u[2 * (p & 1)], u[2 * (p & 1) + 1]); PIN(c.pw[p >> 2]); } }
    SBAR();
    if (XQ) { if (s == 0) mm0(c.sx[tau], c.kf[g], c.qx[g]); else mma(c.sx[tau], c.kf[g], c.qx[g]); SBAR(); }
    if (XE) { f32x4& v = c.sx[g >> 2]; v[g & 3] = ex2(v[g & 3] - c.mx); c.lx += v[g & 3]; PIN(v); PIN(c.lx);
      if (g & 1) { c.pwx[g >> 1] = cvtpk(v[(g & 3) - 1], v[g & 3]); PIN(c.pwx); } SBAR(); }
  }
  if (EXP) { const f32x4& u = c.s[X][1][1]; c.pw[1][3] = cvtpk(u[2], u[3]); }
}
template <int PV, int X, bool MAX, bool XP, bool NK, class F = NoDma> DI void phaseB(St& c, F dma = F{}) {
  PIN(c.pw[1]); SBAR(); NOP2();
  const bf16x8 p0 = __builtin_bit_cast(bf16x8, c.pw[0]), p1 = __builtin_bit_cast(bf16x8, c.pw[1]);
#pragma unroll
  for (int g = 0; g < 8; ++g) {
    const bf16x8 vf = cat(c.vf[g][0], c.vf[g][1]);
    SBAR();
    mma(c.o[0][g], vf, p0);
    SBAR();
    if (g < 4) rdV(c, g + 4, 0);
    if (NK && g >= 4) rdK<PV>(c, g - 4);
    if (MAX && g >= 2 && g < 6) {
      const f32x4& n = c.s[X ^ 1][(g - 2) >> 1][(g - 2) & 1];
      if (g == 2) c.ma = MX3(n[0], n[1], n[2]); else c.ma = MX3(c.ma, n[0], n[1]);
      PIN(c.ma);
    }
    SBAR();
    mma(c.o[1][g], vf, p1);
    SBAR();
    if (g < 4) rdV(c, g + 4, 1);
    dma(g);
    if (MAX && g >= 2 && g < 6) {
      const f32x4& n = c.s[X ^ 1][(g - 2) >> 1][(g - 2) & 1];
      if (g == 2) c.ma = MX3(c.ma, n[3], n[3]); else c.ma = MX3(c.ma, n[2], n[3]);
      PIN(c.ma);
    }
    SBAR();
    if (XP) { mma(c.ox[g], vf, __builtin_bit_cast(bf16x8, c.pwx)); SBAR(); }
    if (g == 0) { mma(c.ls[0], c.ones, p0); SBAR(); }
    if (g == 1) { mma(c.ls[1], c.ones, p1); SBAR(); }
  }
}

DI void ldo(bf16x8& d, unsigned voff, rsrc_t srd, unsigned soff, int imm) {
  asm volatile("buffer_load_dwordx4 %0, %1, %2, %3 offen offset:%4 sc1" : "=v"(d) : "v"(voff), "s"(srd), "s"(soff), "i"(imm) : "memory");
}
DI void sto(bf16x8 v, unsigned voff, rsrc_t srd, unsigned off) { __builtin_amdgcn_raw_buffer_store_b128(__builtin_bit_cast(u32x4, v), srd, voff + off, 0, 16); }
#define XORV(r, k) asm volatile("v_xor_b32 %0, %1, %0" : "+v"(r) : "i"(k))
#define LDSW(T, p) (*(__attribute__((address_space(3))) T*)(p))
#define LDSR(T, p) (*(const __attribute__((address_space(3))) T*)(p))

DI unsigned lane_id() { unsigned l; asm volatile("v_mbcnt_lo_u32_b32 %0, -1, 0\n\tv_mbcnt_hi_u32_b32 %0, -1, %0" : "=v"(l)); return l; }
DI unsigned opq(unsigned x) { asm volatile("" : "+s"(x)); return x; }
DI lds_cptr xor32(lds_cptr p) { return (lds_cptr)(uintptr_t)((unsigned)(uintptr_t)p ^ 32u); }
struct Id { unsigned w, g, hf, par, wq2, grp, jq, br, b, h0; };
DI Id mkid(unsigned wid, unsigned bid) {
  wid = opq(wid); bid = opq(bid);
  Id i; i.w = wid & 3; i.g = wid >> 2; i.hf = wid & 1; i.par = (wid >> 1) & 1; i.wq2 = wid >> 1;
  i.grp = 4 * (bid & 7) + ((bid >> 3) & 3); i.jq = bid >> 5; i.br = i.grp >> 4; i.b = i.grp & 15; i.h0 = 4 * i.jq; return i;
}
struct StageLd {
  rsrc_t srdX, srdW; unsigned voW, voX, m0, m0x;
  DI void init(const Ptrs& P, unsigned wid, unsigned bid, unsigned lds0, unsigned rnd) {
    const Id i = mkid(wid, bid); const unsigned lane = lane_id(), lr = (lane >> 2) & 7, hi = lane >> 5, p4 = lane & 3;
    srdX = mkrsrc((const char*)P.x[i.br] + (size_t)i.b * SQ * DM * 2, SQ * DM * 2);
    srdW = mkrsrc((const char*)P.wq[i.br] + i.h0 * 256 + rnd * 512, DM * NH * DH * 2 - i.h0 * 256 - rnd * 512);
    voW = 8192u * (8 * i.hf + lr) + 16 * (8 * i.wq2 + 4 * hi + (p4 ^ (2 * i.hf)));
    voX = 1024u * (8 * i.hf + 32 * i.wq2 + lr) + 16 * (4 * hi + (p4 ^ (2 * i.hf)));
    m0 = lds0 + 1024 * i.wq2 + 4096 * i.hf; m0x = lds0 + 4096 * i.wq2 + 1024 * i.hf;
  }
  DI void piece(int step, int k) const {
    if (k < 4) glds<false>(m0, wslot(step) + 8192 * k, voW, srdW, 0u, step * (64 * 8192) + k * (16 * 8192));
    else glds<false>(m0x, xslot(step) + 2048 * (k - 4), voX, srdX, 0u, step * 128 + (k - 4) * (16 * 1024));
  }
  DI void operator()(int step) const { _Pragma("unroll") for (int k = 0; k < 6; ++k) piece(step, k); }
};
DI unsigned uhead(const Id& i, unsigned n) { return 4 * (2 * (n & 3) + i.g) + 2 * (n >> 2) + (i.jq >> 2); }
constexpr int woSlot(int s) { return 32768 * ((0x2031 >> (4 * s)) & 3); }
struct WoLd {
  rsrc_t srd; unsigned vo, m0; Id i;
  DI void init(const Ptrs& P, unsigned wid, unsigned bid, unsigned lds0) {
    i = mkid(wid, bid); const unsigned lane = lane_id(), lr = (lane >> 2) & 7, hi = lane >> 5, p4 = lane & 3, blk = (lane >> 4) & 1;
    srd = mkrsrc((const char*)P.wo[i.br] + (i.jq & 3) * 256, 4096 * 1024 - (i.jq & 3) * 256);
    vo = 1024u * (8 * i.par + lr) + 16 * (4 * (2 * i.hf + hi) + (p4 ^ (2 * blk)));
    m0 = lds0 + 16384 * i.g + 2048 * i.par + 1024 * i.hf;
  }
  DI unsigned off(unsigned hs) const { return hs < 16 ? (uhead(i, hs >> 1) * 128 + 64 * (hs & 1)) * 1024 : 0x40000000u; }
  DI void piece(int slot, unsigned so, int k) const { glds<false>(m0, woSlot(slot) + 4096 * k, vo, srd, so, k * (16 * 1024)); }
  DI void operator()(int hs) const { const unsigned so = off(hs); _Pragma("unroll") for (int k = 0; k < 4; ++k) piece(hs & 3, so, k); }
};
#define LANEVARS const int lane = lane_id(), hi = lane >> 5, r16 = lane & 15, g4 = lane >> 4, qp = (lane & 15) >> 2, p4 = lane & 3, blk = (lane >> 4) & 1, lr = (lane >> 2) & 7, \
                 rowoff = 64 * (r16 & 7) + 16 * (g4 ^ (2 * (r16 >> 3))); (void)hi; (void)qp; (void)p4; (void)blk; (void)lr; (void)rowoff
#define IDVARS const Id id_ = mkid(wid, bid); const unsigned w = id_.w, g = id_.g, hf = id_.hf, par = id_.par, wq2 = id_.wq2, grp = id_.grp, jq = id_.jq, br = id_.br, b = id_.b, h0 = id_.h0; \
               (void)w; (void)g; (void)hf; (void)par; (void)wq2; (void)grp; (void)jq; (void)br; (void)b; (void)h0
#define AINL __attribute__((always_inline))
template <int N> using IC = std::integral_constant<int, N>;
DI rsrc_t flag_srd(const Ptrs& P, unsigned grp) { return mkrsrc((const char*)P.ws + WS_CNT + grp * 4096, 4096); }
DI void flag_ld(unsigned& d, rsrc_t srd, unsigned off) { const unsigned l = lane_id(); asm volatile("buffer_load_dword %0, %1, %2, 0 offen sc1" : "+v"(d) : "v"(off + 4 * (l & 7)), "s"(srd) : "memory"); }
DI void flag_echo(unsigned v, rsrc_t srd, unsigned line0, unsigned me) { const unsigned l = lane_id(); __builtin_amdgcn_raw_buffer_store_b32(v, srd, l < 8 ? 128 * (line0 + l) + 4 * me : 0x80000000u, 0, 16); }
DI void flag_wait(rsrc_t srd, unsigned off, unsigned mask, unsigned tag) {
  const unsigned vo = off + 4 * (lane_id() & mask);
#pragma unroll 1
  for (unsigned spins = 0; spins < (1u << 16); ++spins) {
    const unsigned v = __builtin_amdgcn_raw_buffer_load_b32(srd, vo, 0, 16);
    if (__all(v == tag)) break;
    __builtin_amdgcn_s_sleep(1);
  }
  asm volatile("" ::: "memory");
}

__global__ __launch_bounds__(512, 2) void fused_kernel(Ptrs P) {
  extern __shared__ __attribute__((aligned(16))) char smem[];
  unsigned long long pc0; asm volatile("s_getpc_b64 %0" : "=s"(pc0));
  const unsigned wid = rfl(threadIdx.x >> 6), bid = blockIdx.x;
  {
    const rsrc_t sc = mkrsrc((const void*)pc0, ((bid >> 3) & 3) == (wid >> 1) ? CODEWARM : 0);
    asm volatile("s_nop 4\n\ts_mov_b32 m0, %0\n\ts_nop 0\n\tbuffer_load_dword %1, %2, %3 offen lds" :: "s"(wslot(0) + 1024 * (wid >> 1) + 4096 * (wid & 1)), "v"(128u * (threadIdx.x & 63)), "s"(sc), "s"(8192u * wid) : "memory");
    SBAR(); }
  const unsigned lds0 = (unsigned)(uintptr_t)smem;
  const lds_cptr L = (lds_cptr)smem;
  asm volatile("s_nop 4" ::: "memory");
  unsigned tag = 0, fv = 0;
  { StageLd st; st.init(P, wid, bid, lds0, 0);
    IDVARS; LANEVARS;
    if (wid == 0) flag_ld(fv, flag_srd(P, grp), 0);
    st(0);
    const unsigned voE = 1024u * (128 + 8 * hf + lr) + 16 * (4 * hi + (p4 ^ (2 * hf))) + 256 * wq2;
    _Pragma("unroll") for (int i = 0; i < 2; ++i) glds<false>(lds0 + 8192 * hf + 2048 * wq2, XEO + 1024 * i, voE, st.srdX, 0u, 128 * i);
    st(1);
    _Pragma("unroll") for (int k = 0; k < 5; ++k) st.piece(2, k);
    {
      const unsigned wb = bid >> 7, wi = bid & 127, t4 = wid >> 2;
      const rsrc_t sw = mkrsrc(t4 ? P.wq[wb] : P.wo[wb], 4096 * 1024);
      const unsigned tv = t4 ? 1024u * ((lane >> 2) & 7) + 512u + 128u * (lane & 3) : 128u * lane;
      asm volatile("s_mov_b32 m0, %0\n\ts_nop 0\n\tbuffer_load_dword %1, %2, %3 offen lds" :: "s"(st.m0x + xslot(2) + 2048), "v"(tv), "s"(sw), "s"(32768u * wi + 8192u * (wid & 3)) : "memory"); }
    st.piece(2, 5);
  }

#pragma unroll 1
  for (unsigned rnd = 0; rnd < 2; ++rnd) {
    St c;
    IDVARS;
    const rsrc_t srdK = mkrsrc((const char*)P.k[br] + (size_t)(b * NH + h0 + 2 * rnd + g) * SK * DH * 2, SK * DH * 2);
    const rsrc_t srdV = mkrsrc((const char*)P.v[br] + (size_t)(b * NH + h0 + 2 * rnd + g) * SK * DH * 2, SK * DH * 2);
    unsigned voK, voV;
    { LANEVARS; voK = 256u * (8 * par + lr) + 16 * (4 * (2 * hf + hi) + (p4 ^ (2 * par))); voV = 256u * (8 * par + lr) + 16 * (4 * (2 * hf + hi) + (p4 ^ (2 * blk))); }
    const unsigned m0T = lds0 + 16384 * g + 2048 * par + 1024 * hf;
    auto pcK = [&](unsigned tb, int dt, int i) AINL { glds<true>(m0T, 65536 * (dt & 1) + 4096 * i, voK, srdK, tb, dt * 16384 + i * 4096); };
    auto pcV = [&](unsigned mb, unsigned tb, int dt, int i) AINL { glds<true>(mb, 32768 + 65536 * (dt & 1) + 4096 * i, voV, srdV, tb, dt * 16384 + i * 4096); };
    {
      LANEVARS;
      StageLd st; st.init(P, wid, bid, lds0, rnd);
      const unsigned wx = w ^ (2 * g);
      lds_cptr bW[2][4][2];
#pragma unroll
      for (int j = 0; j < 4; ++j) {
        bW[0][j][0] = L + 4096 * g4 + 2048 * g + 512 * (j ^ wx) + 64 * qp + 32 * (g4 & 1) + 8 * p4; bW[0][j][1] = xor32(bW[0][j][0]);
        bW[1][j][0] = bW[0][j][0] + 65536; bW[1][j][1] = bW[0][j][1] + 65536;
        PIN(bW[1][j][0]); PIN(bW[1][j][1]);
      }
      const lds_cptr bX = L + 98304 + 1024 * (4 * w + (r16 >> 3)) + rowoff;
      const lds_cptr bE = L + XEO + 8192 * (r16 >> 3) + rowoff;
      f32x4 qa[2][8], qx[2];
      bf16x4 a[16][2]; bf16x8 xb[2][2], xe[2];
      auto rdA = [&](int step, int m, int t) AINL { a[m][t] = ldstr(bW[wslot(step) >> 16][(m & 7) >> 1][m & 1] + (wslot(step) & 65535) + 16384 * (m >> 3) + 256 * t); };
      auto rdX = [&](int step, int ks, int i) AINL { if (i < 2) xb[ks][i] = ldsb128(bX + (xslot(step) - 98304) + 2048 * i + 512 * ks); else xe[ks] = ldsb128(bE + 1024 * step + 512 * ks); };
      auto conv = [&](int aa, int s) AINL {
        const f32x4 u = qa[aa][2 * s] * C2, v = qa[aa][2 * s + 1] * C2;
        unsigned A0 = cvtpk(u[0], u[1]), A1 = cvtpk(u[2], u[3]), B0 = cvtpk(v[0], v[1]), B1 = cvtpk(v[2], v[3]);
        auto r0 = __builtin_amdgcn_permlane32_swap(A0, B0, false, false); auto r1 = __builtin_amdgcn_permlane32_swap(A1, B1, false, false);
        auto t0 = __builtin_amdgcn_permlane16_swap(r0[0], r0[1], false, false); auto t1 = __builtin_amdgcn_permlane16_swap(r1[0], r1[1], false, false);
        u32x4 q = {t0[0], t1[0], t0[1], t1[1]};
        PIN(q);
        c.qf[aa][s] = __builtin_bit_cast(bf16x8, q);
      };
      if (rnd == 0) WAIT_BAR(13);
      if (rnd == 0 && wid == 0) { PIN(fv); tag = __builtin_amdgcn_readlane(fv, jq) + 1; __builtin_amdgcn_raw_buffer_store_b32(tag, flag_srd(P, grp), lane == 0 ? 4 * jq : 0x80000000u, 0, 16); }
      rdX(0, 0, 0); rdX(0, 0, 1); rdX(0, 0, 2);
#pragma unroll
      for (int m = 0; m < 7; ++m) { rdA(0, m, 0); rdA(0, m, 1); }
      SBAR();
#pragma unroll
      for (int step = 0; step < 8; ++step) {
#pragma unroll
        for (int m = 0; m < 16; ++m) {
          if (m == 10 && step < 7) { if (step == 0) { WAIT_BAR(7); } else if (step == 6) { WAIT_BAR(4); } else { WAIT_BAR(6); } }
          if (m == 12 && step == 7) {
            SBAR(); NOP8(); SBAR();
#pragma unroll
            for (int u = 0; u < 2; ++u) {
              const f32x4 v = qx[u] * C2;
              u32x2 q = {cvtpk(v[0], v[1]), cvtpk(v[2], v[3])};
              LDSW(u32x2, (lds_ptr)smem + QXO + 4096 * g + 1024 * wx + (16 * (2 * u + (g4 >> 1)) + r16) * 16 + 8 * (g4 & 1)) = q;
            }
            WAIT_BAR(4);
          }
          const int np = m < 9 ? m + 7 : m < 10 ? -1 : m == 10 ? 0 : m - 9, nstep = m < 9 ? step : step + 1;
          const bool rd = np >= 0 && nstep < 8, first = step == 0 && m < 8;
          const bf16x8 af = cat(a[m][0], a[m][1]);
          SBAR();
          if (first) mm0(qa[0][m & 7], af, xb[0][0]); else mma(qa[0][m & 7], af, xb[m >> 3][0]);
          SBAR();
          if (rd) { rdA(nstep, np, 0); if (m == 10) rdA(nstep, np, 1); }
          if (step == 7 && m >= 12) { pcK(0u, 1, m - 12); conv(m & 1, (m - 12) >> 1); }
          SBAR();
          if (first) mm0(qa[1][m & 7], af, xb[0][1]); else mma(qa[1][m & 7], af, xb[m >> 3][1]);
          SBAR();
          if (rd) { if (m == 10) { rdA(nstep, np + 1, 0); rdA(nstep, np + 1, 1); } else rdA(nstep, np, 1); }
          if (m >= 2 && m < 5) rdX(step, 1, m - 2);
          if (m >= 11 && m < 14 && step < 7) rdX(step + 1, 0, m - 11);
          if (step < 5 && m >= 10 && (m & 1) == 0) st.piece(step + 3, (m - 10) >> 1);
          if (step >= 1 && step < 6 && m < 6 && (m & 1)) st.piece(step + 2, 3 + (m >> 1));
          if ((step == 5 || step == 6) && m >= 10 && m < 14) { if (step == 5) pcK(0u, 0, m - 10); else pcV(m0T, 0u, 0, m - 10); }
          SBAR();
          if ((m & 7) < 2) { if (first) mm0(qx[m & 1], af, xe[0]); else mma(qx[m & 1], af, xe[m >> 3]); SBAR(); }
        }
      }
      SBAR();
#pragma unroll
      for (int i = 0; i < 4; ++i) pcV(m0T, 0u, 1, i);
      conv(0, 2); conv(1, 2);
      SBAR(); NOP8(); SBAR();
      conv(0, 3); conv(1, 3);
    }

#define TOGK(k) do { XORV(c.bK0, k); XORV(c.bK1, k); } while (0)
#define TOGV(k) do { XORV(c.bV0, k); XORV(c.bV1, k); } while (0)
#define TILE(XC, XN, GLDS_STMT, LASTT) do { \
      TOGK(0x12000); \
      phaseB<PV, 0, true, XC, !LASTT>(c, [&](int q) AINL { GLDS_STMT; }); decide_main<1, false>(c); \
      TOGV(0x2000); \
      phaseA<PV, 1, !LASTT, XN && !LASTT, true, false, 1>(c); \
      TOGK(0x2000); \
      phaseB<PV, 1, !LASTT, false, !LASTT>(c); \
      TOGV(0x12000); \
      if (!LASTT) { decide_main<0, false>(c); if (XN) decide_x(c); phaseA<PV, 0, true, false, true, XN, 2>(c); } \
    } while (0)
    auto attention = [&](auto PARC) AINL {
      constexpr int PV = decltype(PARC)::value;
      constexpr bool XE = PV == 0, XO = !XE;
      { LANEVARS;
        c.bK0 = L + 16384 * g + 8192 * hf + 2048 * (r16 >> 3) + rowoff + 512 * hf;
        c.bK1 = (lds_cptr)(uintptr_t)((unsigned)(uintptr_t)c.bK0 ^ 512u);
        c.bV0 = L + 32768 + 16384 * g + 8192 * hf + 2048 * (g4 >> 1) + 256 * (g4 & 1) + 64 * qp + 32 * (g4 & 1) + 8 * p4;
        c.bV1 = xor32(c.bV0);
        c.bQx = L + QXO + 4096 * g + 1024 * hf + 16 * lane; }
#pragma unroll
      for (int d = 0; d < 8; ++d) { c.o[0][d] = f32x4{}; c.o[1][d] = f32x4{}; c.ox[d] = f32x4{}; }
      c.ls[0] = c.ls[1] = c.nm[0] = c.nm[1] = f32x4{};
#pragma unroll
      for (int d = 0; d < 8; ++d) { PIN(c.o[0][d]); PIN(c.o[1][d]); PIN(c.ox[d]); }
      PIN(c.ls[0]); PIN(c.ls[1]); PIN(c.nm[0]); PIN(c.nm[1]);
      c.mx = -INFINITY; c.lx = 0.f;
      { u32x4 one = {0x3F803F80u, 0x3F803F80u, 0x3F803F80u, 0x3F803F80u}; asm volatile("" : "+v"(one)); c.ones = __builtin_bit_cast(bf16x8, one); }
#pragma unroll
      for (int s = 0; s < 4; ++s) rdK<PV>(c, s);
      SBAR();
      phaseA<PV, 1, true, XE, false, false, 0>(c);
      TOGK(0x2000);
#pragma unroll
      for (int s = 0; s < 4; ++s) rdK<PV>(c, s);
      decide_main<0, true>(c); if (XE) decide_x(c);
      if (rnd == 1 && wid == 0) flag_ld(fv, flag_srd(P, opq(grp)), 0);
      phaseA<PV, 0, true, false, true, XE, 2>(c);
      if (rnd == 1 && wid == 0) { PIN(fv); flag_echo(fv, flag_srd(P, opq(grp)), 1, jq); }
#pragma unroll 1
      for (unsigned tb = 0; tb < 6 * 16384; tb += 2 * 16384) {
        const unsigned mV = tb == 0 ? m0T + 65536 : m0T;
        TILE(XE, XO, { if (q < 4) pcV(mV, tb, 1, q); else pcK(tb, 2, q - 4); }, false);
        TILE(XO, XE, { if (q < 4) pcV(m0T, tb, 2, q); else pcK(tb, 3, q - 4); }, false);
      }
      TILE(XE, XO, { if (q < 4) pcV(m0T, 0u, 7, q); }, false);
      TILE(XO, XE, { if (q == 1) { if (rnd == 0) { StageLd st; st.init(P, wid, bid, lds0, 1); st(0); } else { WoLd wo; wo.init(P, wid, bid, lds0); wo(0); } } }, true);
      SBAR(); NOP8(); SBAR();
      LANEVARS;
      if (rnd == 1 && wid == 0) flag_ld(fv, flag_srd(P, grp), 128 * (1 + jq));
      const lds_ptr scr = (lds_ptr)smem + 65536 * g;
      c.lx = swapadd32(swapadd16(c.lx));
#pragma unroll
      for (int d = 0; d < 8; ++d) LDSW(f32x4, scr + 8192 * w + 1024 * d + 16 * lane) = c.ox[d];
      const f32x4 own0 = LDSR(f32x4, scr + 10240 * w + 16 * lane), own1 = LDSR(f32x4, scr + 10240 * w + 1024 + 16 * lane);
      asm volatile("" ::: "memory");
      { f32x2 ml = {c.mx, c.lx}; LDSW(f32x2, scr + 10240 * w + 8 * lane) = ml; }
      const rsrc_t srdO = mkrsrc((const char*)P.ws + WS_O + (size_t)(opq(grp) * NH + h0 + 2 * rnd + g) * OITEM, OITEM);
#pragma unroll
      for (int a = 0; a < 2; ++a) {
        const float inv = __builtin_amdgcn_rcpf(c.ls[a][0]);
#pragma unroll
        for (int u = 0; u < 4; ++u) sto(pack44(c.o[a][2 * u] * inv, c.o[a][2 * u + 1] * inv), 8192 * w + 16 * lane, srdO, 4096 * a + 1024 * u);
      }
      asm volatile("s_waitcnt lgkmcnt(0)\n\ts_barrier" ::: "memory");
      {
        f32x2 ml[4];
#pragma unroll
        for (int i = 0; i < 4; ++i) ml[i] = LDSR(f32x2, scr + 10240 * i + 8 * r16);
        const float m = fmaxf(fmaxf(ml[0][0], ml[1][0]), fmaxf(ml[2][0], ml[3][0]));
        float f[4], lt = 0.f;
#pragma unroll
        for (int i = 0; i < 4; ++i) { f[i] = ex2(ml[i][0] - m); lt += f[i] * ml[i][1]; }
        const float inv = __builtin_amdgcn_rcpf(lt);
        f32x4 a0 = {}, a1 = {};
#pragma unroll
        for (int i = 0; i < 4; ++i) {
          f32x4 t0 = LDSR(f32x4, scr + 8192 * i + 2048 * w + 16 * lane), t1 = LDSR(f32x4, scr + 8192 * i + 2048 * w + 1024 + 16 * lane);
          if (w == (unsigned)i) { t0 = own0; t1 = own1; }
          a0 += t0 * f[i]; a1 += t1 * f[i];
        }
        sto(pack44(a0 * inv, a1 * inv), 32768 + 1024 * w + 16 * lane, srdO, 0);
      }
      asm volatile("s_waitcnt vmcnt(9)" ::: "memory"); PIN(fv);
      if (rnd == 1 && wid == 0 && !__all(fv == tag)) flag_wait(flag_srd(P, grp), 128 * (1 + jq), 7, tag);
      asm volatile("s_waitcnt lgkmcnt(0)\n\ts_barrier" ::: "memory");
      if (rnd == 0) { StageLd st; st.init(P, wid, bid, lds0, 1); st(1); st(2); glds<false>(LDSB, 0, 0u, st.srdX, 0x40000000u, 0); } else {
       WoLd wo; wo.init(P, wid, bid, lds0); wo(1); wo(2); }
    };
    if ((par ^ g) == 0) attention(IC<0>{}); else attention(IC<1>{});
  }

  IDVARS; LANEVARS;
  const unsigned typ = jq >> 2, ns = jq & 3;
  const rsrc_t srdF = flag_srd(P, grp);
  if (wid == 0) flag_ld(fv, srdF, 0);

  {
    f32x4 acc[2][8] = {}, accx[8] = {};
    unsigned ptag = 0;
    auto unit = [&](auto PARC) AINL {
      constexpr int XV = decltype(PARC)::value;
      WoLd wo; wo.init(P, wid, bid, lds0);
      const rsrc_t srdO = mkrsrc((const char*)P.ws + WS_O + (size_t)grp * NH * OITEM, NH * OITEM);
      const unsigned voO0 = 8192 * w + 1024 * hf + 16 * lane, voO1 = voO0 ^ 1024u, voO2 = voO0 + 4096, voO3 = voO1 + 4096, voOx = 32768 + 1024 * (w ^ (2 * g)) + 16 * lane;
      bf16x8 of[4][5];
      auto ooff = [&](unsigned hs) AINL -> unsigned { return hs < 16 ? uhead(id_, hs >> 1) * OITEM : 0x40000000u; };
      auto pcO = [&](unsigned so, auto SETC, int k) AINL {
        constexpr int set = decltype(SETC)::value;
        if (k < 4) ldo(of[set][k], (k & 1) ? ((k & 2) ? voO3 : voO2) : ((k & 2) ? voO1 : voO0), srdO, so, 2048 * (set & 1));
        else if ((set & 1) == XV) ldo(of[set][4], voOx, srdO, so, 0);
      };
      auto ldO = [&](unsigned hs, auto SETC) AINL { const unsigned so = ooff(hs); _Pragma("unroll") for (int k = 0; k < 5; ++k) pcO(so, SETC, k); };
      const lds_cptr bA0 = L + 8192 * hf + 2048 * (g4 >> 1) + 256 * (g4 & 1) + 64 * qp + 32 * (g4 & 1) + 8 * p4;
#pragma unroll
      for (int d = 0; d < 8; ++d) { PIN(acc[0][d]); PIN(acc[1][d]); PIN(accx[d]); }
      ldO(0, IC<0>{}); ldO(1, IC<1>{}); ldO(2, IC<2>{});
      constexpr int PD = 6;
      bf16x4 a[16][2];
      lds_cptr pA0, pA1, pB0, pB1;
      auto setp = [&](int set) AINL { pA0 = bA0 + 16384 * g + woSlot(set); pA1 = xor32(pA0); pB0 = (lds_cptr)(uintptr_t)((unsigned)(uintptr_t)pA0 ^ 8192u); pB1 = xor32(pB0); };
      auto rdA = [&](int m, int t) AINL { a[m][t] = ldstr(((m >> 3) ? ((m & 1) ? pB1 : pB0) : ((m & 1) ? pA1 : pA0)) + 512 * ((m & 7) >> 1) + 4096 * t); };
#define OWAIT(N, S) do { if (((S) & 1) == XV) asm volatile("s_waitcnt vmcnt(" #N ")\n\ts_barrier" : "+v"(of[S][0]), "+v"(of[S][1]), "+v"(of[S][2]), "+v"(of[S][3]), "+v"(of[S][4]) :: "memory"); \
                         else asm volatile("s_waitcnt vmcnt(" #N ")\n\ts_barrier" : "+v"(of[S][0]), "+v"(of[S][1]), "+v"(of[S][2]), "+v"(of[S][3]) :: "memory"); } while (0)
      auto ustep = [&](unsigned hs, auto SETC) AINL {
        constexpr int set = decltype(SETC)::value, nset = (set + 1) & 3;
        unsigned soO, soW;
#pragma unroll
        for (int m = 0; m < 16; ++m) {
          if (m == 16 - PD) {
            if (set == 0) { if (hs == 4 && wid == 0) flag_wait(srdF, 128 * (9 + jq), 7, tag); OWAIT(9, nset); } else OWAIT(13, nset);
            setp(nset); }
          const bf16x8 af = cat(a[m][0], a[m][1]);
          SBAR();
          mma(acc[0][m & 7], af, of[set][2 * (m >> 3)]);
          SBAR();
          rdA((m + PD) & 15, 0);
          SBAR();
          mma(acc[1][m & 7], af, of[set][2 * (m >> 3) + 1]);
          SBAR();
          rdA((m + PD) & 15, 1);
          if (m == 0) { soO = ooff(hs + 3); asm volatile("" : "+s"(soO)); }
          if (m == 8) { soW = wo.off(hs + 3); asm volatile("" : "+s"(soW)); }
          if (m < 10 && (m & 1)) pcO(soO, IC<(set + 3) & 3>{}, m >> 1); else if (m >= 10 && m != 12 && m != 14) wo.piece((set + 3) & 3, soW, m < 12 ? m - 10 : (m - 9) >> 1);
          SBAR();
          if ((set & 1) == XV && m < 8) { mma(accx[m], af, of[set][4]); SBAR(); }
        }
      };
      setp(0);
#pragma unroll
      for (int m = 0; m < PD; ++m) { rdA(m, 0); rdA(m, 1); }
      OWAIT(0, 0);
      if (wid == 0) { PIN(fv); flag_echo(fv, srdF, 1, jq); flag_echo(fv, srdF, 9, jq); ptag = __builtin_amdgcn_readlane(fv, jq ^ 4); }
#pragma unroll 1
      for (unsigned hs = 0; hs < 16; hs += 4) { ustep(hs, IC<0>{}); ustep(hs + 1, IC<1>{}); ustep(hs + 2, IC<2>{}); ustep(hs + 3, IC<3>{}); }
      asm volatile("s_waitcnt vmcnt(0)" : "+v"(of[0][0]), "+v"(of[0][1]), "+v"(of[0][2]), "+v"(of[0][3]), "+v"(of[1][0]), "+v"(of[1][1]), "+v"(of[1][2]), "+v"(of[1][3]),
                                           "+v"(of[2][0]), "+v"(of[2][1]), "+v"(of[2][2]), "+v"(of[2][3]), "+v"(of[XV][4]), "+v"(of[XV == 0 ? 2 : 1][4]) :: "memory");
      SBAR(); NOP8(); SBAR();
    };
    if ((par ^ g) == 0) unit(IC<0>{}); else unit(IC<1>{});
    asm volatile("s_waitcnt lgkmcnt(0)\n\ts_barrier" ::: "memory");
    const lds_ptr red = (lds_ptr)smem + 8192 * wid + 16 * lane, redx = (lds_ptr)smem + 65536 + 16 * lane;
    f32x4 mine[8];
    if (g == 0) {
#pragma unroll
      for (int i = 0; i < 8; ++i) { LDSW(f32x4, red + 1024 * i) = acc[1][i]; mine[i] = acc[0][i]; PIN(mine[i]); }
    } else {
#pragma unroll
      for (int i = 0; i < 8; ++i) { LDSW(f32x4, red + 1024 * i) = acc[0][i]; mine[i] = acc[1][i]; PIN(mine[i]); }
    }
#pragma unroll
    for (int i = 0; i < 8; ++i) LDSW(f32x4, redx + 8192 * wid + 1024 * i) = accx[i];
    if (typ == 1 && wid == 0) flag_wait(srdF, 128 * (17 + jq), 0, tag);
    asm volatile("s_waitcnt lgkmcnt(0)\n\ts_barrier" ::: "memory");
    const rsrc_t srdP = mkrsrc((const char*)P.ws + WS_PART + (size_t)(grp * 4 + ns) * PARTB, PARTB);
    u32x4 pp[9];
    if (typ == 1) {
#pragma unroll
      for (int i = 0; i < 9; ++i) pp[i] = __builtin_amdgcn_raw_buffer_load_b128(srdP, 9216 * wid + 16 * lane + 1024 * i, 0, 16);
    }
    f32x4 vx = {};
#pragma unroll
    for (int i = 0; i < 8; ++i) vx += LDSR(f32x4, redx + 8192 * i + 1024 * wid);
    { const lds_cptr oth = L + 8192 * (wid ^ 4) + 16 * lane;
#pragma unroll
      for (int i = 0; i < 8; ++i) mine[i] += LDSR(f32x4, oth + 1024 * i); }
    if (typ == 0) {
#pragma unroll
      for (int i = 0; i < 8; ++i) __builtin_amdgcn_raw_buffer_store_b128(__builtin_bit_cast(u32x4, mine[i]), srdP, 9216 * wid + 16 * lane + 1024 * i, 0, 16);
      __builtin_amdgcn_raw_buffer_store_b128(__builtin_bit_cast(u32x4, vx), srdP, 9216 * wid + 16 * lane + 8192, 0, 16);
      asm volatile("s_waitcnt vmcnt(0)\n\ts_barrier" ::: "memory");
      if (wid == 0) __builtin_amdgcn_raw_buffer_store_b32(ptag, srdF, lane == 0 ? 128 * (17 + (jq ^ 4)) : 0x80000000u, 0, 16);
    } else {
      const lds_ptr tile = (lds_ptr)smem + 131072 - 39168;
      asm volatile("s_waitcnt lgkmcnt(0)\n\ts_barrier" ::: "memory");
#pragma unroll
      for (int i = 0; i < 8; ++i) {
        const f32x4 v = mine[i] + __builtin_bit_cast(f32x4, pp[i]);
        u32x2 px = {cvtpk(v[0], v[1]), cvtpk(v[2], v[3])};
        LDSW(u32x2, tile + (32 * w + 16 * g + r16) * 272 + (16 * i + 4 * g4) * 2) = px;
      }
      { const f32x4 v = vx + __builtin_bit_cast(f32x4, pp[8]); u32x2 px = {cvtpk(v[0], v[1]), cvtpk(v[2], v[3])};
        LDSW(u32x2, tile + (128 + r16) * 272 + (16 * wid + 4 * g4) * 2) = px; }
      asm volatile("s_waitcnt lgkmcnt(0)\n\ts_barrier" ::: "memory");
      char* outb = (char*)P.out + ((size_t)b * SQ * 1024 + br * 512 + 128 * ns) * 2;
#pragma unroll
      for (int i = 0; i < 5; ++i) {
        const int row = 32 * i + 4 * wid + g4;
        if (row < SQ) *(u32x4*)(outb + (size_t)row * 2048 + 16 * r16) = LDSR(u32x4, tile + row * 272 + 16 * r16);
      }
    }
  }
}

extern "C" void kernel_launch(void* const* d_in, const int* in_sizes, int n_in, void* d_out, int out_size, void* d_ws, size_t ws_size, hipStream_t stream) {
  (void)in_sizes; (void)n_in; (void)out_size; (void)ws_size;
  Ptrs P;
  for (int br = 0; br < 2; ++br) { P.x[br] = d_in[5 * br]; P.k[br] = d_in[5 * br + 1]; P.v[br] = d_in[5 * br + 2]; P.wq[br] = d_in[5 * br + 3]; P.wo[br] = d_in[5 * br + 4]; }
  P.out = d_out; P.ws = d_ws;
  (void)hipFuncSetAttribute((const void*)fused_kernel, hipFuncAttributeMaxDynamicSharedMemorySize, LDSB);
  hipLaunchKernelGGL(fused_kernel, dim3(256), dim3(512), LDSB, stream, P);
}
```
